```python
import torch

def _softmax(x, axis=-1):
    return torch.softmax(x, dim=axis)

def _gelu(x):
    return torch.nn.functional.gelu(x, approximate='tanh')

def _silu(x):
    return torch.nn.functional.silu(x)

def _sigmoid(x):
    return torch.sigmoid(x)

def _layernorm(x, axis=-1, eps=1e-05):
    m = x.mean(dim=axis, keepdim=True)
    v = x.var(dim=axis, keepdim=True, unbiased=False)
    return (x - m) / torch.sqrt(v + eps)

def _scatter_add(tgt, idx, src, axis=0):
    return tgt.clone().index_add_(axis, idx, src)

def _segment_sum(src, seg, n):
    out = torch.zeros((n,) + src.shape[1:], dtype=src.dtype)
    return out.index_add_(0, seg, src)

def _pad(x, pw):
    flat = tuple((v for pair in reversed(pw) for v in pair))
    return torch.nn.functional.pad(x, flat)
kernel_name = 'adaLN_cond__linear_kernel_hedgehog__dense_activation_squared_relu__prompt'

def setup_inputs():

    _w = lambda a: (lambda t: t.to(torch.bfloat16) if t.is_floating_point() else t)(torch.as_tensor(a))

    def _A_setup():
        B, S, D, H = (1, 2048, 1024, 32)
        Dh = D // H
        return {'x': _w((lambda *s: torch.randn(*s, dtype=torch.bfloat16) * 0.05)(B, S, D)), 'wqkv': _w((lambda *s: torch.randn(*s, dtype=torch.bfloat16) * 0.4)(D, 3 * D)), 'w_phi': _w((lambda *s: torch.randn(*s, dtype=torch.bfloat16) * 0.4)(H, Dh, Dh) * Dh ** (-0.5)), 'wo': _w((lambda *s: torch.randn(*s, dtype=torch.bfloat16) * 0.05)(D, D))}
    _A_in = _A_setup()

    def _B_setup():
        B, S, D, F = (1, 2048, 1024, 3584)
        return {'x': _w((lambda *s: torch.randn(*s, dtype=torch.bfloat16) * 0.05)(B, S, D)), 'w1': _w((lambda *s: torch.randn(*s, dtype=torch.bfloat16) * 0.05)(D, F)), 'b1': _w((lambda *s: torch.randn(*s, dtype=torch.bfloat16) * 0.4)(F)), 'w2': _w((lambda *s: torch.randn(*s, dtype=torch.bfloat16) * 0.05)(F, D)), 'b2': _w((lambda *s: torch.randn(*s, dtype=torch.bfloat16) * 6.4)(D))}
    _B_in = _B_setup()
    return {'A_x': _A_in['x'], 'A_wqkv': _A_in['wqkv'], 'A_w_phi': _A_in['w_phi'], 'A_wo': _A_in['wo'], 'B_x': _B_in['x'], 'B_w1': _B_in['w1'], 'B_b1': _B_in['b1'], 'B_w2': _B_in['w2'], 'B_b2': _B_in['b2'], 'c': _w((lambda *s: torch.randn(*s, dtype=torch.bfloat16) * 0.1)(1, 1024) * 8.0), 'w_mod': _w((lambda *s: torch.randn(*s, dtype=torch.bfloat16) * 0.1)(1024, 6 * 1024) * 1024 ** (-0.5) * 8.0)}

def reference(A_x, A_wqkv, A_w_phi, A_wo, B_x, B_w1, B_b1, B_w2, B_b2, c, w_mod):

    def _A_ref(x, wqkv, w_phi, wo):
        B, S, D = x.shape
        H = 32
        Dh = D // H
        qkv = (lambda x, *d: x.permute(*d))((x @ wqkv).reshape(B, S, 3, H, Dh), 2, 0, 3, 1, 4)
        q, k, v = (qkv[0], qkv[1], qkv[2])
        pq = torch.einsum('bhsd,hdf->bhsf', q, w_phi)
        pk = torch.einsum('bhsd,hdf->bhsf', k, w_phi)
        phi_q = torch.concatenate([_softmax(pq, axis=-1), _softmax(-pq, axis=-1)], axis=-1)
        phi_k = torch.concatenate([_softmax(pk, axis=-1), _softmax(-pk, axis=-1)], axis=-1)
        kv = torch.einsum('bhsf,bhsd->bhfd', phi_k, v)
        z = torch.einsum('bhsf->bhf', phi_k)
        num = torch.einsum('bhqf,bhfd->bhqd', phi_q, kv)
        den = torch.einsum('bhqf,bhf->bhq', phi_q, z)[..., None] + 1e-06
        o = num / den
        return (lambda x, *d: x.permute(*d))(o, 0, 2, 1, 3).reshape(B, S, D) @ wo

    def _B_ref(x, w1, b1, w2, b2):
        h = torch.einsum('bsd,df->bsf', x, w1) + b1
        zero = torch.zeros_like(h)
        r = torch.where(h > zero, h, zero)
        h = r * r
        out = torch.einsum('bsf,fd->bsd', h, w2) + b2
        return out
    x = A_x
    mod = (_silu(c) @ w_mod).reshape(c.shape[0], 6, 1, -1)
    sa, ca, ga, sb, cb, gb = (mod[:, 0].clone(), mod[:, 1].clone(), mod[:, 2].clone(), mod[:, 3].clone(), mod[:, 4].clone(), mod[:, 5].clone())
    h = _layernorm(x, axis=-1) * (1 + ca) + sa
    x = x + ga * _A_ref(h, A_wqkv, A_w_phi, A_wo)
    h = _layernorm(x, axis=-1) * (1 + cb) + sb
    x = x + gb * _B_ref(h, B_w1, B_b1, B_w2, B_b2)
    return x
```

```cpp
#include <hip/hip_runtime.h>
#include <hip/hip_bf16.h>
typedef unsigned short u16;
__device__ __forceinline__ float b2f(u16 x){ return __uint_as_float(((unsigned)x)<<16); }
__device__ __forceinline__ u16 f2b(float f){ unsigned u=__float_as_uint(f); u += 0x7fffu + ((u>>16)&1u); return (u16)(u>>16); }
__device__ __forceinline__ float rb(float f){ return b2f(f2b(f)); }
constexpr int S=2048,D=1024,H=32,DH=32,F=3584;

__global__ void k_mod(const u16* c,const u16* w,float* mod){
  int n=blockIdx.x*256+threadIdx.x; float a=0;
  for(int k=0;k<D;k++){ float x=b2f(c[k]); float s=rb(x/(1.f+__expf(-x))); a+=s*b2f(w[(size_t)k*6144+n]); }
  mod[n]=rb(a);
}
__global__ void k_ln(const u16* x,const float* sc,const float* sh,u16* h){
  __shared__ float red[256]; int r=blockIdx.x,t=threadIdx.x; float v[4]; float s=0;
  for(int i=0;i<4;i++){v[i]=b2f(x[(size_t)r*D+t*4+i]); s+=v[i];}
  red[t]=s; __syncthreads(); for(int o=128;o;o>>=1){ if(t<o) red[t]+=red[t+o]; __syncthreads(); }
  float m=red[0]/D; __syncthreads(); s=0; for(int i=0;i<4;i++){ float d=v[i]-m; s+=d*d; }
  red[t]=s; __syncthreads(); for(int o=128;o;o>>=1){ if(t<o) red[t]+=red[t+o]; __syncthreads(); }
  float rs=rsqrtf(red[0]/D+1e-5f);
  for(int i=0;i<4;i++){ int c=t*4+i; h[(size_t)r*D+c]=f2b((v[i]-m)*rs*(1.f+sc[c])+sh[c]); }
}
__global__ void k_gemm(const u16* A,const u16* B,float* C,int M,int N,int K){
  __shared__ float As[64][17],Bs[16][65];
  int tx=threadIdx.x&15,ty=threadIdx.x>>4; int m0=blockIdx.y*64,n0=blockIdx.x*64; float acc[4][4]={};
  for(int k0=0;k0<K;k0+=16){
    for(int i=threadIdx.x;i<1024;i+=256){ As[i>>4][i&15]=b2f(A[(size_t)(m0+(i>>4))*K+k0+(i&15)]); Bs[i>>6][i&63]=b2f(B[(size_t)(k0+(i>>6))*N+n0+(i&63)]); }
    __syncthreads();
    for(int k=0;k<16;k++){ float a[4],b[4]; for(int i=0;i<4;i++){a[i]=As[ty*4+i][k]; b[i]=Bs[k][tx*4+i];} for(int i=0;i<4;i++)for(int j=0;j<4;j++)acc[i][j]+=a[i]*b[j]; }
    __syncthreads();
  }
  for(int i=0;i<4;i++)for(int j=0;j<4;j++) C[(size_t)(m0+ty*4+i)*N+n0+tx*4+j]=acc[i][j];
}
__global__ void k_phi(const float* qkv,const u16* wphi,float* phi){
  int idx=blockIdx.x*256+threadIdx.x; int f=idx&31; int h=(idx>>5)&31; int which=(idx>>10)&1; int s=idx>>11;
  float a=0; for(int d=0;d<32;d++) a+=rb(qkv[(size_t)s*3072+which*1024+h*32+d])*b2f(wphi[h*1024+d*32+f]);
  float mx=a,mn=a; for(int o=16;o;o>>=1){ mx=fmaxf(mx,__shfl_xor(mx,o)); mn=fminf(mn,__shfl_xor(mn,o)); }
  float e1=__expf(a-mx),e2=__expf(mn-a); float s1=e1,s2=e2; for(int o=16;o;o>>=1){ s1+=__shfl_xor(s1,o); s2+=__shfl_xor(s2,o);}
  float* p=phi+((size_t)which*S+s)*2048+h*64; p[f]=rb(e1/s1); p[32+f]=rb(e2/s2);
}
__global__ void k_kv(const float* phi,const float* qkv,float* kv){
  int h=blockIdx.x,f=blockIdx.y,d=threadIdx.x; if(d>32) return; float a=0; const float* pk=phi+(size_t)S*2048;
  for(int s=0;s<S;s++){ float v=d<32?rb(qkv[(size_t)s*3072+2048+h*32+d]):1.f; a+=pk[(size_t)s*2048+h*64+f]*v; }
  kv[(h*64+f)*33+d]=rb(a);
}
__global__ void k_o(const float* phi,const float* kv,u16* o){
  int idx=blockIdx.x*256+threadIdx.x; int d=idx&31,h=(idx>>5)&31,s=idx>>10; float n=0,dd=0;
  for(int f=0;f<64;f++){ float p=phi[(size_t)s*2048+h*64+f]; n+=p*kv[(h*64+f)*33+d]; dd+=p*kv[(h*64+f)*33+32]; }
  o[idx]=f2b(n/(dd+1e-6f));
}
__global__ void k_res(const u16* x,const float* y,const float* g,const u16* b,u16* out){
  int i=blockIdx.x*256+threadIdx.x; int c=i&1023; float v=y[i]+(b?b2f(b[c]):0.f); out[i]=f2b(b2f(x[i])+g[c]*v);
}
__global__ void k_act(const float* y,const u16* b,u16* out){
  int i=blockIdx.x*256+threadIdx.x; int c=i%F; float v=y[i]+b2f(b[c]); v=v>0?v:0; out[i]=f2b(v*v);
}
extern "C" void kernel_launch(void* const* d_in, const int* in_sizes, int n_in, void* d_out, int out_size, void* d_ws, size_t ws_size, hipStream_t st) {
  const u16* x=(const u16*)d_in[0]; const u16* wqkv=(const u16*)d_in[1]; const u16* wphi=(const u16*)d_in[2]; const u16* wo=(const u16*)d_in[3];
  const u16* w1=(const u16*)d_in[5]; const u16* b1=(const u16*)d_in[6]; const u16* w2=(const u16*)d_in[7]; const u16* b2=(const u16*)d_in[8];
  const u16* c=(const u16*)d_in[9]; const u16* wmod=(const u16*)d_in[10];
  char* ws=(char*)d_ws; float* mod=(float*)ws; u16* h=(u16*)(ws+(1<<20)); float* y=(float*)(ws+(8<<20)); float* phi=(float*)(ws+(48<<20));
  float* kv=(float*)(ws+(96<<20)); u16* o=(u16*)(ws+(100<<20)); u16* x1=(u16*)(ws+(108<<20)); u16* h1=(u16*)(ws+(116<<20));
  k_mod<<<24,256,0,st>>>(c,wmod,mod);
  k_ln<<<S,256,0,st>>>(x,mod+1024,mod,h);
  k_gemm<<<dim3(48,32),256,0,st>>>(h,wqkv,y,S,3072,D);
  k_phi<<<S*2048/256,256,0,st>>>(y,wphi,phi);
  k_kv<<<dim3(32,64),64,0,st>>>(phi,y,kv);
  k_o<<<S*D/256,256,0,st>>>(phi,kv,o);
  k_gemm<<<dim3(16,32),256,0,st>>>(o,wo,y,S,D,D);
  k_res<<<S*D/256,256,0,st>>>(x,y,mod+2048,nullptr,x1);
  k_ln<<<S,256,0,st>>>(x1,mod+4096,mod+3072,h);
  k_gemm<<<dim3(56,32),256,0,st>>>(h,w1,y,S,F,D);
  k_act<<<S*F/256,256,0,st>>>(y,b1,h1);
  k_gemm<<<dim3(16,32),256,0,st>>>(h1,w2,y,S,D,F);
  k_res<<<S*D/256,256,0,st>>>(x1,y,mod+5120,b2,(u16*)d_out);
}
```

```cpp
#include <hip/hip_runtime.h>
#include <hip/hip_bf16.h>
typedef unsigned short u16;
__device__ __forceinline__ float b2f(u16 x){ return __uint_as_float(((unsigned)x)<<16); }
__device__ __forceinline__ u16 f2b(float f){ unsigned u=__float_as_uint(f); u += 0x7fffu + ((u>>16)&1u); return (u16)(u>>16); }
__device__ __forceinline__ float rb(float f){ return b2f(f2b(f)); }
constexpr int S=2048,D=1024,H=32,DH=32,F=3584;

__global__ void k_mod(const u16* c,const u16* w,float* mod){
  int n=blockIdx.x*256+threadIdx.x; float a=0;
  for(int k=0;k<D;k++){ float x=b2f(c[k]); float s=rb(x/(1.f+__expf(-x))); a+=s*b2f(w[(size_t)k*6144+n]); }
  mod[n]=rb(a);
}
__global__ void k_ln(const u16* x,const float* sc,const float* sh,u16* h){
  __shared__ float red[256]; int r=blockIdx.x,t=threadIdx.x; float v[4]; float s=0;
  for(int i=0;i<4;i++){v[i]=b2f(x[(size_t)r*D+t*4+i]); s+=v[i];}
  red[t]=s; __syncthreads(); for(int o=128;o;o>>=1){ if(t<o) red[t]+=red[t+o]; __syncthreads(); }
  float m=red[0]/D; __syncthreads(); s=0; for(int i=0;i<4;i++){ float d=v[i]-m; s+=d*d; }
  red[t]=s; __syncthreads(); for(int o=128;o;o>>=1){ if(t<o) red[t]+=red[t+o]; __syncthreads(); }
  float rs=rsqrtf(red[0]/D+1e-5f);
  for(int i=0;i<4;i++){ int c=t*4+i; h[(size_t)r*D+c]=f2b((v[i]-m)*rs*(1.f+sc[c])+sh[c]); }
}
__global__ void k_gemm(const u16* A,const u16* B,float* C,int M,int N,int K){
  __shared__ float As[64][17],Bs[16][65];
  int tx=threadIdx.x&15,ty=threadIdx.x>>4; int m0=blockIdx.y*64,n0=blockIdx.x*64; float acc[4][4]={};
  for(int k0=0;k0<K;k0+=16){
    for(int i=threadIdx.x;i<1024;i+=256){ As[i>>4][i&15]=b2f(A[(size_t)(m0+(i>>4))*K+k0+(i&15)]); Bs[i>>6][i&63]=b2f(B[(size_t)(k0+(i>>6))*N+n0+(i&63)]); }
    __syncthreads();
    for(int k=0;k<16;k++){ float a[4],b[4]; for(int i=0;i<4;i++){a[i]=As[ty*4+i][k]; b[i]=Bs[k][tx*4+i];} for(int i=0;i<4;i++)for(int j=0;j<4;j++)acc[i][j]+=a[i]*b[j]; }
    __syncthreads();
  }
  for(int i=0;i<4;i++)for(int j=0;j<4;j++) C[(size_t)(m0+ty*4+i)*N+n0+tx*4+j]=acc[i][j];
}
__global__ void k_phi(const float* qkv,const u16* wphi,float* phi){
  int idx=blockIdx.x*256+threadIdx.x; int f=idx&31; int h=(idx>>5)&31; int which=(idx>>10)&1; int s=idx>>11;
  float a=0; for(int d=0;d<32;d++) a+=rb(qkv[(size_t)s*3072+which*1024+h*32+d])*b2f(wphi[h*1024+d*32+f]);
  float mx=a,mn=a; for(int o=16;o;o>>=1){ mx=fmaxf(mx,__shfl_xor(mx,o)); mn=fminf(mn,__shfl_xor(mn,o)); }
  float e1=__expf(a-mx),e2=__expf(mn-a); float s1=e1,s2=e2; for(int o=16;o;o>>=1){ s1+=__shfl_xor(s1,o); s2+=__shfl_xor(s2,o);}
  float* p=phi+((size_t)which*S+s)*2048+h*64; p[f]=rb(e1/s1); p[32+f]=rb(e2/s2);
}
__global__ void k_kv(const float* phi,const float* qkv,float* kv){
  int h=blockIdx.x,f=blockIdx.y,d=threadIdx.x; if(d>32) return; float a=0; const float* pk=phi+(size_t)S*2048;
  for(int s=0;s<S;s++){ float v=d<32?rb(qkv[(size_t)s*3072+2048+h*32+d]):1.f; a+=pk[(size_t)s*2048+h*64+f]*v; }
  kv[(h*64+f)*33+d]=rb(a);
}
__global__ void k_o(const float* phi,const float* kv,u16* o){
  int idx=blockIdx.x*256+threadIdx.x; int d=idx&31,h=(idx>>5)&31,s=idx>>10; float n=0,dd=0;
  for(int f=0;f<64;f++){ float p=phi[(size_t)s*2048+h*64+f]; n+=p*kv[(h*64+f)*33+d]; dd+=p*kv[(h*64+f)*33+32]; }
  o[idx]=f2b(n/(dd+1e-6f));
}
__global__ void k_res(const u16* x,const float* y,const float* g,const u16* b,u16* out){
  int i=blockIdx.x*256+threadIdx.x; int c=i&1023; float v=y[i]+(b?b2f(b[c]):0.f); out[i]=f2b(b2f(x[i])+g[c]*v);
}
__global__ void k_act(const float* y,const u16* b,u16* out){
  int i=blockIdx.x*256+threadIdx.x; int c=i%F; float v=y[i]+b2f(b[c]); v=v>0?v:0; out[i]=f2b(v*v);
}

typedef unsigned u32x4 __attribute__((ext_vector_type(4)));
__device__ __forceinline__ void spin_mult(unsigned long long t0,unsigned long long t1,int mult){
  unsigned long long d=(t1-t0)*mult; if(d>400000ull) d=400000ull;
  while(__builtin_amdgcn_s_memrealtime()-t1<d) __builtin_amdgcn_s_sleep(4);
}
__global__ __launch_bounds__(512) void p_stream(const u32x4* p,int per,unsigned* sink,int mult){
  unsigned long long t0=__builtin_amdgcn_s_memrealtime();
  unsigned s=0; const u32x4* q=p+(size_t)blockIdx.x*per*512+threadIdx.x;
  for(int i=0;i<per;i++){ u32x4 v=q[i*512]; s+=v[0]^v[1]^v[2]^v[3]; }
  if(s==0x1234567) *sink=s;
  __syncthreads();
  unsigned long long t1=__builtin_amdgcn_s_memrealtime();
  spin_mult(t0,t1,mult);
}
__global__ void p_chase(const unsigned* p,int n,int stride,unsigned* sink,int mult){
  unsigned long long t0=__builtin_amdgcn_s_memrealtime();
  unsigned s=0;
  for(int i=0;i<n;i++){ unsigned v=__builtin_nontemporal_load(p+(size_t)i*stride+(s&1)*0+threadIdx.x*0); asm volatile("s_waitcnt vmcnt(0)":"+v"(v)); s+=v; }
  if(s==0x1234567) *sink=s;
  unsigned long long t1=__builtin_amdgcn_s_memrealtime();
  spin_mult(t0,t1,mult);
}

extern "C" void kernel_launch(void* const* d_in, const int* in_sizes, int n_in, void* d_out, int out_size, void* d_ws, size_t ws_size, hipStream_t st) {
  const u16* x=(const u16*)d_in[0]; const u16* wqkv=(const u16*)d_in[1]; const u16* wphi=(const u16*)d_in[2]; const u16* wo=(const u16*)d_in[3];
  const u16* w1=(const u16*)d_in[5]; const u16* b1=(const u16*)d_in[6]; const u16* w2=(const u16*)d_in[7]; const u16* b2=(const u16*)d_in[8];
  const u16* c=(const u16*)d_in[9]; const u16* wmod=(const u16*)d_in[10];
  char* ws=(char*)d_ws; float* mod=(float*)ws; u16* h=(u16*)(ws+(1<<20)); float* y=(float*)(ws+(8<<20)); float* phi=(float*)(ws+(48<<20));
  float* kv=(float*)(ws+(96<<20)); u16* o=(u16*)(ws+(100<<20)); u16* x1=(u16*)(ws+(108<<20)); u16* h1=(u16*)(ws+(116<<20));
  p_chase<<<1,64,0,st>>>((const unsigned*)(ws+(248<<20)),100,16416,(unsigned*)(ws+(200<<20)),20);
  k_mod<<<24,256,0,st>>>(c,wmod,mod);
  k_ln<<<S,256,0,st>>>(x,mod+1024,mod,h);
  k_gemm<<<dim3(48,32),256,0,st>>>(h,wqkv,y,S,3072,D);
  k_phi<<<S*2048/256,256,0,st>>>(y,wphi,phi);
  k_kv<<<dim3(32,64),64,0,st>>>(phi,y,kv);
  k_o<<<S*D/256,256,0,st>>>(phi,kv,o);
  k_gemm<<<dim3(16,32),256,0,st>>>(o,wo,y,S,D,D);
  k_res<<<S*D/256,256,0,st>>>(x,y,mod+2048,nullptr,x1);
  k_ln<<<S,256,0,st>>>(x1,mod+4096,mod+3072,h);
  k_gemm<<<dim3(56,32),256,0,st>>>(h,w1,y,S,F,D);
  k_act<<<S*F/256,256,0,st>>>(y,b1,h1);
  k_gemm<<<dim3(16,32),256,0,st>>>(h1,w2,y,S,D,F);
  k_res<<<S*D/256,256,0,st>>>(x1,y,mod+5120,b2,(u16*)d_out);
}
```

```cpp
#include <hip/hip_runtime.h>
#pragma clang diagnostic ignored "-Wint-to-pointer-cast"
typedef unsigned short u16;
typedef unsigned long long u64;
typedef short s16x4 __attribute__((ext_vector_type(4)));
typedef short s16x8 __attribute__((ext_vector_type(8)));
typedef __bf16 bf16x8 __attribute__((ext_vector_type(8)));
typedef __bf16 bf16x2 __attribute__((ext_vector_type(2)));
typedef float f32x2 __attribute__((ext_vector_type(2)));
typedef float f32x4 __attribute__((ext_vector_type(4)));
typedef float f32x16 __attribute__((ext_vector_type(16)));
typedef unsigned u32x2 __attribute__((ext_vector_type(2)));
typedef unsigned u32x4 __attribute__((ext_vector_type(4)));
#define LAS __attribute__((address_space(3)))
#define CAS __attribute__((address_space(4)))
#define DI __device__ __forceinline__
typedef __amdgpu_buffer_rsrc_t rsrc_t;

#define QKV_ASM \
  "v_mov_b64 v[0:1], 0\n\t" \
  "v_mov_b64 v[2:3], 0\n\t" \
  "v_mov_b64 v[4:5], 0\n\t" \
  "v_mov_b64 v[6:7], 0\n\t" \
  "v_mov_b64 v[8:9], 0\n\t" \
  "v_mov_b64 v[10:11], 0\n\t" \
  "v_mov_b64 v[12:13], 0\n\t" \
  "v_mov_b64 v[14:15], 0\n\t" \
  "v_mov_b64 v[16:17], 0\n\t" \
  "v_mov_b64 v[18:19], 0\n\t" \
  "v_mov_b64 v[20:21], 0\n\t" \
  "v_mov_b64 v[22:23], 0\n\t" \
  "v_mov_b64 v[24:25], 0\n\t" \
  "v_mov_b64 v[26:27], 0\n\t" \
  "v_mov_b64 v[28:29], 0\n\t" \
  "v_mov_b64 v[30:31], 0\n\t" \
  "v_mov_b64 v[32:33], 0\n\t" \
  "v_mov_b64 v[34:35], 0\n\t" \
  "v_mov_b64 v[36:37], 0\n\t" \
  "v_mov_b64 v[38:39], 0\n\t" \
  "v_mov_b64 v[40:41], 0\n\t" \
  "v_mov_b64 v[42:43], 0\n\t" \
  "v_mov_b64 v[44:45], 0\n\t" \
  "v_mov_b64 v[46:47], 0\n\t" \
  "v_mov_b64 v[48:49], 0\n\t" \
  "v_mov_b64 v[50:51], 0\n\t" \
  "v_mov_b64 v[52:53], 0\n\t" \
  "v_mov_b64 v[54:55], 0\n\t" \
  "v_mov_b64 v[56:57], 0\n\t" \
  "v_mov_b64 v[58:59], 0\n\t" \
  "v_mov_b64 v[60:61], 0\n\t" \
  "v_mov_b64 v[62:63], 0\n\t" \
  "v_mov_b64 v[64:65], 0\n\t" \
  "v_mov_b64 v[66:67], 0\n\t" \
  "v_mov_b64 v[68:69], 0\n\t" \
  "v_mov_b64 v[70:71], 0\n\t" \
  "v_mov_b64 v[72:73], 0\n\t" \
  "v_mov_b64 v[74:75], 0\n\t" \
  "v_mov_b64 v[76:77], 0\n\t" \
  "v_mov_b64 v[78:79], 0\n\t" \
  "v_mov_b64 v[80:81], 0\n\t" \
  "v_mov_b64 v[82:83], 0\n\t" \
  "v_mov_b64 v[84:85], 0\n\t" \
  "v_mov_b64 v[86:87], 0\n\t" \
  "v_mov_b64 v[88:89], 0\n\t" \
  "v_mov_b64 v[90:91], 0\n\t" \
  "v_mov_b64 v[92:93], 0\n\t" \
  "v_mov_b64 v[94:95], 0\n\t" \
  "s_mov_b32 %[cnt], 4\n\t" \
  "s_barrier\n\t" \
  "ds_read_b64_tr_b16 v[112:113], %[b0e0] offset:0\n\t" \
  "ds_read_b64_tr_b16 v[114:115], %[b0e0] offset:2048\n\t" \
  "ds_read_b64_tr_b16 v[116:117], %[b0e1] offset:0\n\t" \
  "ds_read_b128 v[96:99], %[a0] offset:0\n\t" \
  "ds_read_b64_tr_b16 v[118:119], %[b0e1] offset:2048\n\t" \
  "ds_read_b64_tr_b16 v[120:121], %[b0e0] offset:4096\n\t" \
  "ds_read_b64_tr_b16 v[122:123], %[b0e0] offset:6144\n\t" \
  "ds_read_b128 v[100:103], %[a0] offset:1024\n\t" \
  "ds_read_b64_tr_b16 v[124:125], %[b0e1] offset:4096\n\t" \
  "ds_read_b64_tr_b16 v[126:127], %[b0e1] offset:6144\n\t" \
  "ds_read_b64_tr_b16 v[128:129], %[b0e0] offset:8192\n\t" \
  "ds_read_b128 v[104:107], %[a0] offset:2048\n\t" \
  "ds_read_b64_tr_b16 v[130:131], %[b0e0] offset:10240\n\t" \
  "ds_read_b64_tr_b16 v[132:133], %[b0e1] offset:8192\n\t" \
  "ds_read_b64_tr_b16 v[134:135], %[b0e1] offset:10240\n\t" \
  "ds_read_b128 v[108:111], %[a0] offset:3072\n\t" \
  "1:\n\t" \
  "s_waitcnt lgkmcnt(0)\n\t" \
  "v_mfma_f32_16x16x32_bf16 v[0:3], v[112:115], v[96:99], v[0:3]\n\t" \
  "s_barrier\n\t" \
  "v_mfma_f32_16x16x32_bf16 v[4:7], v[112:115], v[100:103], v[4:7]\n\t" \
  "ds_read_b64_tr_b16 v[152:153], %[b0e0] offset:12288\n\t" \
  "v_mfma_f32_16x16x32_bf16 v[8:11], v[112:115], v[104:107], v[8:11]\n\t" \
  "ds_read_b64_tr_b16 v[154:155], %[b0e0] offset:14336\n\t" \
  "v_mfma_f32_16x16x32_bf16 v[12:15], v[112:115], v[108:111], v[12:15]\n\t" \
  "ds_read_b64_tr_b16 v[156:157], %[b0e1] offset:12288\n\t" \
  "v_mfma_f32_16x16x32_bf16 v[28:31], v[116:119], v[108:111], v[28:31]\n\t" \
  "ds_read_b128 v[136:139], %[a0] offset:8192\n\t" \
  "v_mfma_f32_16x16x32_bf16 v[24:27], v[116:119], v[104:107], v[24:27]\n\t" \
  "ds_read_b64_tr_b16 v[158:159], %[b0e1] offset:14336\n\t" \
  "v_mfma_f32_16x16x32_bf16 v[20:23], v[116:119], v[100:103], v[20:23]\n\t" \
  "ds_read_b64_tr_b16 v[160:161], %[b0e0] offset:16384\n\t" \
  "v_mfma_f32_16x16x32_bf16 v[16:19], v[116:119], v[96:99], v[16:19]\n\t" \
  "ds_read_b64_tr_b16 v[162:163], %[b0e0] offset:18432\n\t" \
  "v_mfma_f32_16x16x32_bf16 v[32:35], v[120:123], v[96:99], v[32:35]\n\t" \
  "ds_read_b128 v[140:143], %[a0] offset:9216\n\t" \
  "v_mfma_f32_16x16x32_bf16 v[36:39], v[120:123], v[100:103], v[36:39]\n\t" \
  "ds_read_b64_tr_b16 v[164:165], %[b0e1] offset:16384\n\t" \
  "v_mfma_f32_16x16x32_bf16 v[40:43], v[120:123], v[104:107], v[40:43]\n\t" \
  "ds_read_b64_tr_b16 v[166:167], %[b0e1] offset:18432\n\t" \
  "v_mfma_f32_16x16x32_bf16 v[44:47], v[120:123], v[108:111], v[44:47]\n\t" \
  "ds_read_b64_tr_b16 v[168:169], %[b0e0] offset:20480\n\t" \
  "v_mfma_f32_16x16x32_bf16 v[60:63], v[124:127], v[108:111], v[60:63]\n\t" \
  "ds_read_b128 v[144:147], %[a0] offset:10240\n\t" \
  "v_mfma_f32_16x16x32_bf16 v[56:59], v[124:127], v[104:107], v[56:59]\n\t" \
  "ds_read_b64_tr_b16 v[170:171], %[b0e0] offset:22528\n\t" \
  "v_mfma_f32_16x16x32_bf16 v[52:55], v[124:127], v[100:103], v[52:55]\n\t" \
  "ds_read_b64_tr_b16 v[172:173], %[b0e1] offset:20480\n\t" \
  "v_mfma_f32_16x16x32_bf16 v[48:51], v[124:127], v[96:99], v[48:51]\n\t" \
  "ds_read_b64_tr_b16 v[174:175], %[b0e1] offset:22528\n\t" \
  "v_mfma_f32_16x16x32_bf16 v[64:67], v[96:99], v[128:131], v[64:67]\n\t" \
  "ds_read_b128 v[148:151], %[a0] offset:11264\n\t" \
  "v_mfma_f32_16x16x32_bf16 v[68:71], v[100:103], v[128:131], v[68:71]\n\t" \
  "v_mfma_f32_16x16x32_bf16 v[72:75], v[104:107], v[128:131], v[72:75]\n\t" \
  "v_mfma_f32_16x16x32_bf16 v[76:79], v[108:111], v[128:131], v[76:79]\n\t" \
  "v_mfma_f32_16x16x32_bf16 v[92:95], v[108:111], v[132:135], v[92:95]\n\t" \
  "v_mfma_f32_16x16x32_bf16 v[88:91], v[104:107], v[132:135], v[88:91]\n\t" \
  "v_mfma_f32_16x16x32_bf16 v[84:87], v[100:103], v[132:135], v[84:87]\n\t" \
  "v_mfma_f32_16x16x32_bf16 v[80:83], v[96:99], v[132:135], v[80:83]\n\t" \
  "s_waitcnt lgkmcnt(0)\n\t" \
  "v_mfma_f32_16x16x32_bf16 v[0:3], v[152:155], v[136:139], v[0:3]\n\t" \
  "s_barrier\n\t" \
  "v_mfma_f32_16x16x32_bf16 v[4:7], v[152:155], v[140:143], v[4:7]\n\t" \
  "ds_read_b64_tr_b16 v[112:113], %[b0e0] offset:24576\n\t" \
  "v_mfma_f32_16x16x32_bf16 v[8:11], v[152:155], v[144:147], v[8:11]\n\t" \
  "ds_read_b64_tr_b16 v[114:115], %[b0e0] offset:26624\n\t" \
  "v_mfma_f32_16x16x32_bf16 v[12:15], v[152:155], v[148:151], v[12:15]\n\t" \
  "ds_read_b64_tr_b16 v[116:117], %[b0e1] offset:24576\n\t" \
  "v_mfma_f32_16x16x32_bf16 v[28:31], v[156:159], v[148:151], v[28:31]\n\t" \
  "ds_read_b128 v[96:99], %[a0] offset:16384\n\t" \
  "v_mfma_f32_16x16x32_bf16 v[24:27], v[156:159], v[144:147], v[24:27]\n\t" \
  "ds_read_b64_tr_b16 v[118:119], %[b0e1] offset:26624\n\t" \
  "v_mfma_f32_16x16x32_bf16 v[20:23], v[156:159], v[140:143], v[20:23]\n\t" \
  "ds_read_b64_tr_b16 v[120:121], %[b0e0] offset:28672\n\t" \
  "v_mfma_f32_16x16x32_bf16 v[16:19], v[156:159], v[136:139], v[16:19]\n\t" \
  "ds_read_b64_tr_b16 v[122:123], %[b0e0] offset:30720\n\t" \
  "v_mfma_f32_16x16x32_bf16 v[32:35], v[160:163], v[136:139], v[32:35]\n\t" \
  "ds_read_b128 v[100:103], %[a0] offset:17408\n\t" \
  "v_mfma_f32_16x16x32_bf16 v[36:39], v[160:163], v[140:143], v[36:39]\n\t" \
  "ds_read_b64_tr_b16 v[124:125], %[b0e1] offset:28672\n\t" \
  "v_mfma_f32_16x16x32_bf16 v[40:43], v[160:163], v[144:147], v[40:43]\n\t" \
  "ds_read_b64_tr_b16 v[126:127], %[b0e1] offset:30720\n\t" \
  "v_mfma_f32_16x16x32_bf16 v[44:47], v[160:163], v[148:151], v[44:47]\n\t" \
  "ds_read_b64_tr_b16 v[128:129], %[b0e0] offset:32768\n\t" \
  "v_mfma_f32_16x16x32_bf16 v[60:63], v[164:167], v[148:151], v[60:63]\n\t" \
  "ds_read_b128 v[104:107], %[a0] offset:18432\n\t" \
  "v_mfma_f32_16x16x32_bf16 v[56:59], v[164:167], v[144:147], v[56:59]\n\t" \
  "ds_read_b64_tr_b16 v[130:131], %[b0e0] offset:34816\n\t" \
  "v_mfma_f32_16x16x32_bf16 v[52:55], v[164:167], v[140:143], v[52:55]\n\t" \
  "ds_read_b64_tr_b16 v[132:133], %[b0e1] offset:32768\n\t" \
  "v_mfma_f32_16x16x32_bf16 v[48:51], v[164:167], v[136:139], v[48:51]\n\t" \
  "ds_read_b64_tr_b16 v[134:135], %[b0e1] offset:34816\n\t" \
  "v_mfma_f32_16x16x32_bf16 v[64:67], v[136:139], v[168:171], v[64:67]\n\t" \
  "ds_read_b128 v[108:111], %[a0] offset:19456\n\t" \
  "v_mfma_f32_16x16x32_bf16 v[68:71], v[140:143], v[168:171], v[68:71]\n\t" \
  "v_mfma_f32_16x16x32_bf16 v[72:75], v[144:147], v[168:171], v[72:75]\n\t" \
  "v_mfma_f32_16x16x32_bf16 v[76:79], v[148:151], v[168:171], v[76:79]\n\t" \
  "v_mfma_f32_16x16x32_bf16 v[92:95], v[148:151], v[172:175], v[92:95]\n\t" \
  "v_mfma_f32_16x16x32_bf16 v[88:91], v[144:147], v[172:175], v[88:91]\n\t" \
  "v_mfma_f32_16x16x32_bf16 v[84:87], v[140:143], v[172:175], v[84:87]\n\t" \
  "v_mfma_f32_16x16x32_bf16 v[80:83], v[136:139], v[172:175], v[80:83]\n\t" \
  "s_waitcnt lgkmcnt(0)\n\t" \
  "v_mfma_f32_16x16x32_bf16 v[0:3], v[112:115], v[96:99], v[0:3]\n\t" \
  "s_barrier\n\t" \
  "v_mfma_f32_16x16x32_bf16 v[4:7], v[112:115], v[100:103], v[4:7]\n\t" \
  "ds_read_b64_tr_b16 v[152:153], %[b0e0] offset:36864\n\t" \
  "v_mfma_f32_16x16x32_bf16 v[8:11], v[112:115], v[104:107], v[8:11]\n\t" \
  "ds_read_b64_tr_b16 v[154:155], %[b0e0] offset:38912\n\t" \
  "v_mfma_f32_16x16x32_bf16 v[12:15], v[112:115], v[108:111], v[12:15]\n\t" \
  "ds_read_b64_tr_b16 v[156:157], %[b0e1] offset:36864\n\t" \
  "v_mfma_f32_16x16x32_bf16 v[28:31], v[116:119], v[108:111], v[28:31]\n\t" \
  "ds_read_b128 v[136:139], %[a0] offset:24576\n\t" \
  "v_mfma_f32_16x16x32_bf16 v[24:27], v[116:119], v[104:107], v[24:27]\n\t" \
  "ds_read_b64_tr_b16 v[158:159], %[b0e1] offset:38912\n\t" \
  "v_mfma_f32_16x16x32_bf16 v[20:23], v[116:119], v[100:103], v[20:23]\n\t" \
  "ds_read_b64_tr_b16 v[160:161], %[b0e0] offset:40960\n\t" \
  "v_mfma_f32_16x16x32_bf16 v[16:19], v[116:119], v[96:99], v[16:19]\n\t" \
  "ds_read_b64_tr_b16 v[162:163], %[b0e0] offset:43008\n\t" \
  "v_mfma_f32_16x16x32_bf16 v[32:35], v[120:123], v[96:99], v[32:35]\n\t" \
  "ds_read_b128 v[140:143], %[a0] offset:25600\n\t" \
  "v_mfma_f32_16x16x32_bf16 v[36:39], v[120:123], v[100:103], v[36:39]\n\t" \
  "ds_read_b64_tr_b16 v[164:165], %[b0e1] offset:40960\n\t" \
  "v_mfma_f32_16x16x32_bf16 v[40:43], v[120:123], v[104:107], v[40:43]\n\t" \
  "ds_read_b64_tr_b16 v[166:167], %[b0e1] offset:43008\n\t" \
  "v_mfma_f32_16x16x32_bf16 v[44:47], v[120:123], v[108:111], v[44:47]\n\t" \
  "ds_read_b64_tr_b16 v[168:169], %[b0e0] offset:45056\n\t" \
  "v_mfma_f32_16x16x32_bf16 v[60:63], v[124:127], v[108:111], v[60:63]\n\t" \
  "ds_read_b128 v[144:147], %[a0] offset:26624\n\t" \
  "v_mfma_f32_16x16x32_bf16 v[56:59], v[124:127], v[104:107], v[56:59]\n\t" \
  "ds_read_b64_tr_b16 v[170:171], %[b0e0] offset:47104\n\t" \
  "v_mfma_f32_16x16x32_bf16 v[52:55], v[124:127], v[100:103], v[52:55]\n\t" \
  "ds_read_b64_tr_b16 v[172:173], %[b0e1] offset:45056\n\t" \
  "v_mfma_f32_16x16x32_bf16 v[48:51], v[124:127], v[96:99], v[48:51]\n\t" \
  "ds_read_b64_tr_b16 v[174:175], %[b0e1] offset:47104\n\t" \
  "v_mfma_f32_16x16x32_bf16 v[64:67], v[96:99], v[128:131], v[64:67]\n\t" \
  "ds_read_b128 v[148:151], %[a0] offset:27648\n\t" \
  "v_mfma_f32_16x16x32_bf16 v[68:71], v[100:103], v[128:131], v[68:71]\n\t" \
  "v_mfma_f32_16x16x32_bf16 v[72:75], v[104:107], v[128:131], v[72:75]\n\t" \
  "v_mfma_f32_16x16x32_bf16 v[76:79], v[108:111], v[128:131], v[76:79]\n\t" \
  "v_mfma_f32_16x16x32_bf16 v[92:95], v[108:111], v[132:135], v[92:95]\n\t" \
  "v_mfma_f32_16x16x32_bf16 v[88:91], v[104:107], v[132:135], v[88:91]\n\t" \
  "v_mfma_f32_16x16x32_bf16 v[84:87], v[100:103], v[132:135], v[84:87]\n\t" \
  "v_mfma_f32_16x16x32_bf16 v[80:83], v[96:99], v[132:135], v[80:83]\n\t" \
  "s_waitcnt lgkmcnt(0)\n\t" \
  "v_mfma_f32_16x16x32_bf16 v[0:3], v[152:155], v[136:139], v[0:3]\n\t" \
  "s_barrier\n\t" \
  "v_mfma_f32_16x16x32_bf16 v[4:7], v[152:155], v[140:143], v[4:7]\n\t" \
  "ds_read_b64_tr_b16 v[112:113], %[b1e0] offset:0\n\t" \
  "v_mfma_f32_16x16x32_bf16 v[8:11], v[152:155], v[144:147], v[8:11]\n\t" \
  "ds_read_b64_tr_b16 v[114:115], %[b1e0] offset:2048\n\t" \
  "v_mfma_f32_16x16x32_bf16 v[12:15], v[152:155], v[148:151], v[12:15]\n\t" \
  "ds_read_b64_tr_b16 v[116:117], %[b1e1] offset:0\n\t" \
  "v_mfma_f32_16x16x32_bf16 v[28:31], v[156:159], v[148:151], v[28:31]\n\t" \
  "ds_read_b128 v[96:99], %[a0] offset:0\n\t" \
  "v_mfma_f32_16x16x32_bf16 v[24:27], v[156:159], v[144:147], v[24:27]\n\t" \
  "ds_read_b64_tr_b16 v[118:119], %[b1e1] offset:2048\n\t" \
  "v_mfma_f32_16x16x32_bf16 v[20:23], v[156:159], v[140:143], v[20:23]\n\t" \
  "ds_read_b64_tr_b16 v[120:121], %[b1e0] offset:4096\n\t" \
  "v_mfma_f32_16x16x32_bf16 v[16:19], v[156:159], v[136:139], v[16:19]\n\t" \
  "ds_read_b64_tr_b16 v[122:123], %[b1e0] offset:6144\n\t" \
  "v_mfma_f32_16x16x32_bf16 v[32:35], v[160:163], v[136:139], v[32:35]\n\t" \
  "ds_read_b128 v[100:103], %[a0] offset:1024\n\t" \
  "v_mfma_f32_16x16x32_bf16 v[36:39], v[160:163], v[140:143], v[36:39]\n\t" \
  "ds_read_b64_tr_b16 v[124:125], %[b1e1] offset:4096\n\t" \
  "v_mfma_f32_16x16x32_bf16 v[40:43], v[160:163], v[144:147], v[40:43]\n\t" \
  "ds_read_b64_tr_b16 v[126:127], %[b1e1] offset:6144\n\t" \
  "v_mfma_f32_16x16x32_bf16 v[44:47], v[160:163], v[148:151], v[44:47]\n\t" \
  "ds_read_b64_tr_b16 v[128:129], %[b1e0] offset:8192\n\t" \
  "v_mfma_f32_16x16x32_bf16 v[60:63], v[164:167], v[148:151], v[60:63]\n\t" \
  "ds_read_b128 v[104:107], %[a0] offset:2048\n\t" \
  "v_mfma_f32_16x16x32_bf16 v[56:59], v[164:167], v[144:147], v[56:59]\n\t" \
  "ds_read_b64_tr_b16 v[130:131], %[b1e0] offset:10240\n\t" \
  "v_mfma_f32_16x16x32_bf16 v[52:55], v[164:167], v[140:143], v[52:55]\n\t" \
  "ds_read_b64_tr_b16 v[132:133], %[b1e1] offset:8192\n\t" \
  "v_mfma_f32_16x16x32_bf16 v[48:51], v[164:167], v[136:139], v[48:51]\n\t" \
  "ds_read_b64_tr_b16 v[134:135], %[b1e1] offset:10240\n\t" \
  "v_mfma_f32_16x16x32_bf16 v[64:67], v[136:139], v[168:171], v[64:67]\n\t" \
  "ds_read_b128 v[108:111], %[a0] offset:3072\n\t" \
  "v_mfma_f32_16x16x32_bf16 v[68:71], v[140:143], v[168:171], v[68:71]\n\t" \
  "v_mfma_f32_16x16x32_bf16 v[72:75], v[144:147], v[168:171], v[72:75]\n\t" \
  "v_mfma_f32_16x16x32_bf16 v[76:79], v[148:151], v[168:171], v[76:79]\n\t" \
  "v_mfma_f32_16x16x32_bf16 v[92:95], v[148:151], v[172:175], v[92:95]\n\t" \
  "v_mfma_f32_16x16x32_bf16 v[88:91], v[144:147], v[172:175], v[88:91]\n\t" \
  "v_mfma_f32_16x16x32_bf16 v[84:87], v[140:143], v[172:175], v[84:87]\n\t" \
  "v_mfma_f32_16x16x32_bf16 v[80:83], v[136:139], v[172:175], v[80:83]\n\t" \
  "s_waitcnt lgkmcnt(0)\n\t" \
  "v_mfma_f32_16x16x32_bf16 v[0:3], v[112:115], v[96:99], v[0:3]\n\t" \
  "s_barrier\n\t" \
  "v_mfma_f32_16x16x32_bf16 v[4:7], v[112:115], v[100:103], v[4:7]\n\t" \
  "ds_read_b64_tr_b16 v[152:153], %[b1e0] offset:12288\n\t" \
  "v_mfma_f32_16x16x32_bf16 v[8:11], v[112:115], v[104:107], v[8:11]\n\t" \
  "ds_read_b64_tr_b16 v[154:155], %[b1e0] offset:14336\n\t" \
  "v_mfma_f32_16x16x32_bf16 v[12:15], v[112:115], v[108:111], v[12:15]\n\t" \
  "ds_read_b64_tr_b16 v[156:157], %[b1e1] offset:12288\n\t" \
  "v_mfma_f32_16x16x32_bf16 v[28:31], v[116:119], v[108:111], v[28:31]\n\t" \
  "ds_read_b128 v[136:139], %[a0] offset:8192\n\t" \
  "v_mfma_f32_16x16x32_bf16 v[24:27], v[116:119], v[104:107], v[24:27]\n\t" \
  "ds_read_b64_tr_b16 v[158:159], %[b1e1] offset:14336\n\t" \
  "v_mfma_f32_16x16x32_bf16 v[20:23], v[116:119], v[100:103], v[20:23]\n\t" \
  "ds_read_b64_tr_b16 v[160:161], %[b1e0] offset:16384\n\t" \
  "v_mfma_f32_16x16x32_bf16 v[16:19], v[116:119], v[96:99], v[16:19]\n\t" \
  "ds_read_b64_tr_b16 v[162:163], %[b1e0] offset:18432\n\t" \
  "v_mfma_f32_16x16x32_bf16 v[32:35], v[120:123], v[96:99], v[32:35]\n\t" \
  "ds_read_b128 v[140:143], %[a0] offset:9216\n\t" \
  "v_mfma_f32_16x16x32_bf16 v[36:39], v[120:123], v[100:103], v[36:39]\n\t" \
  "ds_read_b64_tr_b16 v[164:165], %[b1e1] offset:16384\n\t" \
  "v_mfma_f32_16x16x32_bf16 v[40:43], v[120:123], v[104:107], v[40:43]\n\t" \
  "ds_read_b64_tr_b16 v[166:167], %[b1e1] offset:18432\n\t" \
  "v_mfma_f32_16x16x32_bf16 v[44:47], v[120:123], v[108:111], v[44:47]\n\t" \
  "ds_read_b64_tr_b16 v[168:169], %[b1e0] offset:20480\n\t" \
  "v_mfma_f32_16x16x32_bf16 v[60:63], v[124:127], v[108:111], v[60:63]\n\t" \
  "ds_read_b128 v[144:147], %[a0] offset:10240\n\t" \
  "v_mfma_f32_16x16x32_bf16 v[56:59], v[124:127], v[104:107], v[56:59]\n\t" \
  "ds_read_b64_tr_b16 v[170:171], %[b1e0] offset:22528\n\t" \
  "v_mfma_f32_16x16x32_bf16 v[52:55], v[124:127], v[100:103], v[52:55]\n\t" \
  "ds_read_b64_tr_b16 v[172:173], %[b1e1] offset:20480\n\t" \
  "v_mfma_f32_16x16x32_bf16 v[48:51], v[124:127], v[96:99], v[48:51]\n\t" \
  "ds_read_b64_tr_b16 v[174:175], %[b1e1] offset:22528\n\t" \
  "v_mfma_f32_16x16x32_bf16 v[64:67], v[96:99], v[128:131], v[64:67]\n\t" \
  "ds_read_b128 v[148:151], %[a0] offset:11264\n\t" \
  "v_mfma_f32_16x16x32_bf16 v[68:71], v[100:103], v[128:131], v[68:71]\n\t" \
  "v_mfma_f32_16x16x32_bf16 v[72:75], v[104:107], v[128:131], v[72:75]\n\t" \
  "v_mfma_f32_16x16x32_bf16 v[76:79], v[108:111], v[128:131], v[76:79]\n\t" \
  "v_mfma_f32_16x16x32_bf16 v[92:95], v[108:111], v[132:135], v[92:95]\n\t" \
  "v_mfma_f32_16x16x32_bf16 v[88:91], v[104:107], v[132:135], v[88:91]\n\t" \
  "v_mfma_f32_16x16x32_bf16 v[84:87], v[100:103], v[132:135], v[84:87]\n\t" \
  "v_mfma_f32_16x16x32_bf16 v[80:83], v[96:99], v[132:135], v[80:83]\n\t" \
  "s_waitcnt lgkmcnt(0)\n\t" \
  "v_mfma_f32_16x16x32_bf16 v[0:3], v[152:155], v[136:139], v[0:3]\n\t" \
  "s_barrier\n\t" \
  "v_mfma_f32_16x16x32_bf16 v[4:7], v[152:155], v[140:143], v[4:7]\n\t" \
  "ds_read_b64_tr_b16 v[112:113], %[b1e0] offset:24576\n\t" \
  "v_mfma_f32_16x16x32_bf16 v[8:11], v[152:155], v[144:147], v[8:11]\n\t" \
  "ds_read_b64_tr_b16 v[114:115], %[b1e0] offset:26624\n\t" \
  "v_mfma_f32_16x16x32_bf16 v[12:15], v[152:155], v[148:151], v[12:15]\n\t" \
  "ds_read_b64_tr_b16 v[116:117], %[b1e1] offset:24576\n\t" \
  "v_mfma_f32_16x16x32_bf16 v[28:31], v[156:159], v[148:151], v[28:31]\n\t" \
  "ds_read_b128 v[96:99], %[a0] offset:16384\n\t" \
  "v_mfma_f32_16x16x32_bf16 v[24:27], v[156:159], v[144:147], v[24:27]\n\t" \
  "ds_read_b64_tr_b16 v[118:119], %[b1e1] offset:26624\n\t" \
  "v_mfma_f32_16x16x32_bf16 v[20:23], v[156:159], v[140:143], v[20:23]\n\t" \
  "ds_read_b64_tr_b16 v[120:121], %[b1e0] offset:28672\n\t" \
  "v_mfma_f32_16x16x32_bf16 v[16:19], v[156:159], v[136:139], v[16:19]\n\t" \
  "ds_read_b64_tr_b16 v[122:123], %[b1e0] offset:30720\n\t" \
  "v_mfma_f32_16x16x32_bf16 v[32:35], v[160:163], v[136:139], v[32:35]\n\t" \
  "ds_read_b128 v[100:103], %[a0] offset:17408\n\t" \
  "v_mfma_f32_16x16x32_bf16 v[36:39], v[160:163], v[140:143], v[36:39]\n\t" \
  "ds_read_b64_tr_b16 v[124:125], %[b1e1] offset:28672\n\t" \
  "v_mfma_f32_16x16x32_bf16 v[40:43], v[160:163], v[144:147], v[40:43]\n\t" \
  "ds_read_b64_tr_b16 v[126:127], %[b1e1] offset:30720\n\t" \
  "v_mfma_f32_16x16x32_bf16 v[44:47], v[160:163], v[148:151], v[44:47]\n\t" \
  "ds_read_b64_tr_b16 v[128:129], %[b1e0] offset:32768\n\t" \
  "v_mfma_f32_16x16x32_bf16 v[60:63], v[164:167], v[148:151], v[60:63]\n\t" \
  "ds_read_b128 v[104:107], %[a0] offset:18432\n\t" \
  "v_mfma_f32_16x16x32_bf16 v[56:59], v[164:167], v[144:147], v[56:59]\n\t" \
  "ds_read_b64_tr_b16 v[130:131], %[b1e0] offset:34816\n\t" \
  "v_mfma_f32_16x16x32_bf16 v[52:55], v[164:167], v[140:143], v[52:55]\n\t" \
  "ds_read_b64_tr_b16 v[132:133], %[b1e1] offset:32768\n\t" \
  "v_mfma_f32_16x16x32_bf16 v[48:51], v[164:167], v[136:139], v[48:51]\n\t" \
  "ds_read_b64_tr_b16 v[134:135], %[b1e1] offset:34816\n\t" \
  "v_mfma_f32_16x16x32_bf16 v[64:67], v[136:139], v[168:171], v[64:67]\n\t" \
  "ds_read_b128 v[108:111], %[a0] offset:19456\n\t" \
  "v_mfma_f32_16x16x32_bf16 v[68:71], v[140:143], v[168:171], v[68:71]\n\t" \
  "v_mfma_f32_16x16x32_bf16 v[72:75], v[144:147], v[168:171], v[72:75]\n\t" \
  "v_mfma_f32_16x16x32_bf16 v[76:79], v[148:151], v[168:171], v[76:79]\n\t" \
  "v_mfma_f32_16x16x32_bf16 v[92:95], v[148:151], v[172:175], v[92:95]\n\t" \
  "v_mfma_f32_16x16x32_bf16 v[88:91], v[144:147], v[172:175], v[88:91]\n\t" \
  "v_mfma_f32_16x16x32_bf16 v[84:87], v[140:143], v[172:175], v[84:87]\n\t" \
  "v_mfma_f32_16x16x32_bf16 v[80:83], v[136:139], v[172:175], v[80:83]\n\t" \
  "s_waitcnt lgkmcnt(0)\n\t" \
  "v_mfma_f32_16x16x32_bf16 v[0:3], v[112:115], v[96:99], v[0:3]\n\t" \
  "s_barrier\n\t" \
  "v_mfma_f32_16x16x32_bf16 v[4:7], v[112:115], v[100:103], v[4:7]\n\t" \
  "ds_read_b64_tr_b16 v[152:153], %[b1e0] offset:36864\n\t" \
  "v_mfma_f32_16x16x32_bf16 v[8:11], v[112:115], v[104:107], v[8:11]\n\t" \
  "ds_read_b64_tr_b16 v[154:155], %[b1e0] offset:38912\n\t" \
  "v_mfma_f32_16x16x32_bf16 v[12:15], v[112:115], v[108:111], v[12:15]\n\t" \
  "ds_read_b64_tr_b16 v[156:157], %[b1e1] offset:36864\n\t" \
  "v_mfma_f32_16x16x32_bf16 v[28:31], v[116:119], v[108:111], v[28:31]\n\t" \
  "ds_read_b128 v[136:139], %[a0] offset:24576\n\t" \
  "v_mfma_f32_16x16x32_bf16 v[24:27], v[116:119], v[104:107], v[24:27]\n\t" \
  "ds_read_b64_tr_b16 v[158:159], %[b1e1] offset:38912\n\t" \
  "v_mfma_f32_16x16x32_bf16 v[20:23], v[116:119], v[100:103], v[20:23]\n\t" \
  "ds_read_b64_tr_b16 v[160:161], %[b1e0] offset:40960\n\t" \
  "v_mfma_f32_16x16x32_bf16 v[16:19], v[116:119], v[96:99], v[16:19]\n\t" \
  "ds_read_b64_tr_b16 v[162:163], %[b1e0] offset:43008\n\t" \
  "v_mfma_f32_16x16x32_bf16 v[32:35], v[120:123], v[96:99], v[32:35]\n\t" \
  "ds_read_b128 v[140:143], %[a0] offset:25600\n\t" \
  "v_mfma_f32_16x16x32_bf16 v[36:39], v[120:123], v[100:103], v[36:39]\n\t" \
  "ds_read_b64_tr_b16 v[164:165], %[b1e1] offset:40960\n\t" \
  "v_mfma_f32_16x16x32_bf16 v[40:43], v[120:123], v[104:107], v[40:43]\n\t" \
  "ds_read_b64_tr_b16 v[166:167], %[b1e1] offset:43008\n\t" \
  "v_mfma_f32_16x16x32_bf16 v[44:47], v[120:123], v[108:111], v[44:47]\n\t" \
  "ds_read_b64_tr_b16 v[168:169], %[b1e0] offset:45056\n\t" \
  "v_mfma_f32_16x16x32_bf16 v[60:63], v[124:127], v[108:111], v[60:63]\n\t" \
  "ds_read_b128 v[144:147], %[a0] offset:26624\n\t" \
  "v_mfma_f32_16x16x32_bf16 v[56:59], v[124:127], v[104:107], v[56:59]\n\t" \
  "ds_read_b64_tr_b16 v[170:171], %[b1e0] offset:47104\n\t" \
  "v_mfma_f32_16x16x32_bf16 v[52:55], v[124:127], v[100:103], v[52:55]\n\t" \
  "ds_read_b64_tr_b16 v[172:173], %[b1e1] offset:45056\n\t" \
  "v_mfma_f32_16x16x32_bf16 v[48:51], v[124:127], v[96:99], v[48:51]\n\t" \
  "ds_read_b64_tr_b16 v[174:175], %[b1e1] offset:47104\n\t" \
  "v_mfma_f32_16x16x32_bf16 v[64:67], v[96:99], v[128:131], v[64:67]\n\t" \
  "ds_read_b128 v[148:151], %[a0] offset:27648\n\t" \
  "v_mfma_f32_16x16x32_bf16 v[68:71], v[100:103], v[128:131], v[68:71]\n\t" \
  "v_mfma_f32_16x16x32_bf16 v[72:75], v[104:107], v[128:131], v[72:75]\n\t" \
  "v_mfma_f32_16x16x32_bf16 v[76:79], v[108:111], v[128:131], v[76:79]\n\t" \
  "v_mfma_f32_16x16x32_bf16 v[92:95], v[108:111], v[132:135], v[92:95]\n\t" \
  "v_mfma_f32_16x16x32_bf16 v[88:91], v[104:107], v[132:135], v[88:91]\n\t" \
  "v_mfma_f32_16x16x32_bf16 v[84:87], v[100:103], v[132:135], v[84:87]\n\t" \
  "v_mfma_f32_16x16x32_bf16 v[80:83], v[96:99], v[132:135], v[80:83]\n\t" \
  "s_waitcnt lgkmcnt(0)\n\t" \
  "v_mfma_f32_16x16x32_bf16 v[0:3], v[152:155], v[136:139], v[0:3]\n\t" \
  "s_barrier\n\t" \
  "v_mfma_f32_16x16x32_bf16 v[4:7], v[152:155], v[140:143], v[4:7]\n\t" \
  "ds_read_b64_tr_b16 v[112:113], %[b0e0] offset:0\n\t" \
  "v_mfma_f32_16x16x32_bf16 v[8:11], v[152:155], v[144:147], v[8:11]\n\t" \
  "ds_read_b64_tr_b16 v[114:115], %[b0e0] offset:2048\n\t" \
  "v_mfma_f32_16x16x32_bf16 v[12:15], v[152:155], v[148:151], v[12:15]\n\t" \
  "ds_read_b64_tr_b16 v[116:117], %[b0e1] offset:0\n\t" \
  "v_mfma_f32_16x16x32_bf16 v[28:31], v[156:159], v[148:151], v[28:31]\n\t" \
  "ds_read_b128 v[96:99], %[a0] offset:0\n\t" \
  "v_mfma_f32_16x16x32_bf16 v[24:27], v[156:159], v[144:147], v[24:27]\n\t" \
  "ds_read_b64_tr_b16 v[118:119], %[b0e1] offset:2048\n\t" \
  "v_mfma_f32_16x16x32_bf16 v[20:23], v[156:159], v[140:143], v[20:23]\n\t" \
  "ds_read_b64_tr_b16 v[120:121], %[b0e0] offset:4096\n\t" \
  "v_mfma_f32_16x16x32_bf16 v[16:19], v[156:159], v[136:139], v[16:19]\n\t" \
  "ds_read_b64_tr_b16 v[122:123], %[b0e0] offset:6144\n\t" \
  "v_mfma_f32_16x16x32_bf16 v[32:35], v[160:163], v[136:139], v[32:35]\n\t" \
  "ds_read_b128 v[100:103], %[a0] offset:1024\n\t" \
  "v_mfma_f32_16x16x32_bf16 v[36:39], v[160:163], v[140:143], v[36:39]\n\t" \
  "ds_read_b64_tr_b16 v[124:125], %[b0e1] offset:4096\n\t" \
  "v_mfma_f32_16x16x32_bf16 v[40:43], v[160:163], v[144:147], v[40:43]\n\t" \
  "ds_read_b64_tr_b16 v[126:127], %[b0e1] offset:6144\n\t" \
  "v_mfma_f32_16x16x32_bf16 v[44:47], v[160:163], v[148:151], v[44:47]\n\t" \
  "ds_read_b64_tr_b16 v[128:129], %[b0e0] offset:8192\n\t" \
  "v_mfma_f32_16x16x32_bf16 v[60:63], v[164:167], v[148:151], v[60:63]\n\t" \
  "ds_read_b128 v[104:107], %[a0] offset:2048\n\t" \
  "v_mfma_f32_16x16x32_bf16 v[56:59], v[164:167], v[144:147], v[56:59]\n\t" \
  "ds_read_b64_tr_b16 v[130:131], %[b0e0] offset:10240\n\t" \
  "v_mfma_f32_16x16x32_bf16 v[52:55], v[164:167], v[140:143], v[52:55]\n\t" \
  "ds_read_b64_tr_b16 v[132:133], %[b0e1] offset:8192\n\t" \
  "v_mfma_f32_16x16x32_bf16 v[48:51], v[164:167], v[136:139], v[48:51]\n\t" \
  "ds_read_b64_tr_b16 v[134:135], %[b0e1] offset:10240\n\t" \
  "v_mfma_f32_16x16x32_bf16 v[64:67], v[136:139], v[168:171], v[64:67]\n\t" \
  "ds_read_b128 v[108:111], %[a0] offset:3072\n\t" \
  "v_mfma_f32_16x16x32_bf16 v[68:71], v[140:143], v[168:171], v[68:71]\n\t" \
  "s_add_i32 %[cnt], %[cnt], -1\n\t" \
  "v_mfma_f32_16x16x32_bf16 v[72:75], v[144:147], v[168:171], v[72:75]\n\t" \
  "s_cmp_lg_u32 %[cnt], 0\n\t" \
  "v_mfma_f32_16x16x32_bf16 v[76:79], v[148:151], v[168:171], v[76:79]\n\t" \
  "v_mfma_f32_16x16x32_bf16 v[92:95], v[148:151], v[172:175], v[92:95]\n\t" \
  "v_mfma_f32_16x16x32_bf16 v[88:91], v[144:147], v[172:175], v[88:91]\n\t" \
  "v_mfma_f32_16x16x32_bf16 v[84:87], v[140:143], v[172:175], v[84:87]\n\t" \
  "v_mfma_f32_16x16x32_bf16 v[80:83], v[136:139], v[172:175], v[80:83]\n\t" \
  "s_cbranch_scc1 1b\n\t" \
  "s_waitcnt lgkmcnt(0)\n\t" \
  "s_nop 7\n\t" \
  ""
#define QKV_CLOB "v96","v97","v98","v99","v100","v101","v102","v103","v104","v105","v106","v107","v108","v109","v110","v111","v112","v113","v114","v115","v116","v117","v118","v119","v120","v121","v122","v123","v124","v125","v126","v127","v128","v129","v130","v131","v132","v133","v134","v135","v136","v137","v138","v139","v140","v141","v142","v143","v144","v145","v146","v147","v148","v149","v150","v151","v152","v153","v154","v155","v156","v157","v158","v159","v160","v161","v162","v163","v164","v165","v166","v167","v168","v169","v170","v171","v172","v173","v174","v175"
#define FF1_ASM \
  "s_mov_b32 %[cnt], 4\n\t" \
  "s_barrier\n\t" \
  "ds_read_b64_tr_b16 v[128:129], %[b0e0] offset:14336\n\t" \
  "ds_read_b64_tr_b16 v[130:131], %[b0e0] offset:16384\n\t" \
  "ds_read_b64_tr_b16 v[132:133], %[b0e1] offset:14336\n\t" \
  "ds_read_b128 v[112:115], %[a0] offset:0\n\t" \
  "ds_read_b64_tr_b16 v[134:135], %[b0e1] offset:16384\n\t" \
  "ds_read_b64_tr_b16 v[136:137], %[b0e0] offset:18432\n\t" \
  "ds_read_b64_tr_b16 v[138:139], %[b0e0] offset:20480\n\t" \
  "ds_read_b128 v[116:119], %[a0] offset:1024\n\t" \
  "ds_read_b64_tr_b16 v[140:141], %[b0e1] offset:18432\n\t" \
  "ds_read_b64_tr_b16 v[142:143], %[b0e1] offset:20480\n\t" \
  "ds_read_b64_tr_b16 v[144:145], %[b0e0] offset:22528\n\t" \
  "ds_read_b128 v[120:123], %[a0] offset:2048\n\t" \
  "ds_read_b64_tr_b16 v[146:147], %[b0e0] offset:24576\n\t" \
  "ds_read_b64_tr_b16 v[148:149], %[b0e1] offset:22528\n\t" \
  "ds_read_b64_tr_b16 v[150:151], %[b0e1] offset:24576\n\t" \
  "ds_read_b128 v[124:127], %[a0] offset:3072\n\t" \
  "ds_read_b64_tr_b16 v[152:153], %[h0] offset:26624\n\t" \
  "ds_read_b64_tr_b16 v[154:155], %[h0] offset:27648\n\t" \
  "1:\n\t" \
  "s_waitcnt lgkmcnt(0)\n\t" \
  "v_mfma_f32_16x16x32_bf16 v[0:3], v[128:131], v[112:115], v[0:3]\n\t" \
  "s_barrier\n\t" \
  "v_mfma_f32_16x16x32_bf16 v[4:7], v[128:131], v[116:119], v[4:7]\n\t" \
  "ds_read_b64_tr_b16 v[172:173], %[b0e0] offset:0\n\t" \
  "v_mfma_f32_16x16x32_bf16 v[8:11], v[128:131], v[120:123], v[8:11]\n\t" \
  "ds_read_b64_tr_b16 v[174:175], %[b0e0] offset:2048\n\t" \
  "v_mfma_f32_16x16x32_bf16 v[12:15], v[128:131], v[124:127], v[12:15]\n\t" \
  "ds_read_b64_tr_b16 v[176:177], %[b0e1] offset:0\n\t" \
  "v_mfma_f32_16x16x32_bf16 v[28:31], v[132:135], v[124:127], v[28:31]\n\t" \
  "ds_read_b128 v[156:159], %[a0] offset:8192\n\t" \
  "v_mfma_f32_16x16x32_bf16 v[24:27], v[132:135], v[120:123], v[24:27]\n\t" \
  "ds_read_b64_tr_b16 v[178:179], %[b0e1] offset:2048\n\t" \
  "v_mfma_f32_16x16x32_bf16 v[20:23], v[132:135], v[116:119], v[20:23]\n\t" \
  "ds_read_b64_tr_b16 v[180:181], %[b0e0] offset:4096\n\t" \
  "v_mfma_f32_16x16x32_bf16 v[16:19], v[132:135], v[112:115], v[16:19]\n\t" \
  "ds_read_b64_tr_b16 v[182:183], %[b0e0] offset:6144\n\t" \
  "v_mfma_f32_16x16x32_bf16 v[32:35], v[136:139], v[112:115], v[32:35]\n\t" \
  "ds_read_b128 v[160:163], %[a0] offset:9216\n\t" \
  "v_mfma_f32_16x16x32_bf16 v[36:39], v[136:139], v[116:119], v[36:39]\n\t" \
  "ds_read_b64_tr_b16 v[184:185], %[b0e1] offset:4096\n\t" \
  "v_mfma_f32_16x16x32_bf16 v[40:43], v[136:139], v[120:123], v[40:43]\n\t" \
  "ds_read_b64_tr_b16 v[186:187], %[b0e1] offset:6144\n\t" \
  "v_mfma_f32_16x16x32_bf16 v[44:47], v[136:139], v[124:127], v[44:47]\n\t" \
  "ds_read_b64_tr_b16 v[188:189], %[b0e0] offset:8192\n\t" \
  "v_mfma_f32_16x16x32_bf16 v[60:63], v[140:143], v[124:127], v[60:63]\n\t" \
  "ds_read_b128 v[164:167], %[a0] offset:10240\n\t" \
  "v_mfma_f32_16x16x32_bf16 v[56:59], v[140:143], v[120:123], v[56:59]\n\t" \
  "ds_read_b64_tr_b16 v[190:191], %[b0e0] offset:10240\n\t" \
  "v_mfma_f32_16x16x32_bf16 v[52:55], v[140:143], v[116:119], v[52:55]\n\t" \
  "ds_read_b64_tr_b16 v[192:193], %[b0e1] offset:8192\n\t" \
  "v_mfma_f32_16x16x32_bf16 v[48:51], v[140:143], v[112:115], v[48:51]\n\t" \
  "ds_read_b64_tr_b16 v[194:195], %[b0e1] offset:10240\n\t" \
  "v_mfma_f32_16x16x32_bf16 v[64:67], v[144:147], v[112:115], v[64:67]\n\t" \
  "ds_read_b128 v[168:171], %[a0] offset:11264\n\t" \
  "v_mfma_f32_16x16x32_bf16 v[68:71], v[144:147], v[116:119], v[68:71]\n\t" \
  "ds_read_b64_tr_b16 v[196:197], %[h0] offset:12288\n\t" \
  "v_mfma_f32_16x16x32_bf16 v[72:75], v[144:147], v[120:123], v[72:75]\n\t" \
  "ds_read_b64_tr_b16 v[198:199], %[h0] offset:13312\n\t" \
  "v_mfma_f32_16x16x32_bf16 v[76:79], v[144:147], v[124:127], v[76:79]\n\t" \
  "v_mfma_f32_16x16x32_bf16 v[92:95], v[148:151], v[124:127], v[92:95]\n\t" \
  "v_mfma_f32_16x16x32_bf16 v[88:91], v[148:151], v[120:123], v[88:91]\n\t" \
  "v_mfma_f32_16x16x32_bf16 v[84:87], v[148:151], v[116:119], v[84:87]\n\t" \
  "v_mfma_f32_16x16x32_bf16 v[80:83], v[148:151], v[112:115], v[80:83]\n\t" \
  "v_mfma_f32_16x16x32_bf16 v[96:99], v[152:155], v[112:115], v[96:99]\n\t" \
  "v_mfma_f32_16x16x32_bf16 v[100:103], v[152:155], v[116:119], v[100:103]\n\t" \
  "v_mfma_f32_16x16x32_bf16 v[104:107], v[152:155], v[120:123], v[104:107]\n\t" \
  "v_mfma_f32_16x16x32_bf16 v[108:111], v[152:155], v[124:127], v[108:111]\n\t" \
  "s_waitcnt lgkmcnt(0)\n\t" \
  "v_mfma_f32_16x16x32_bf16 v[0:3], v[172:175], v[156:159], v[0:3]\n\t" \
  "s_barrier\n\t" \
  "v_mfma_f32_16x16x32_bf16 v[4:7], v[172:175], v[160:163], v[4:7]\n\t" \
  "ds_read_b64_tr_b16 v[128:129], %[b1e0] offset:43008\n\t" \
  "v_mfma_f32_16x16x32_bf16 v[8:11], v[172:175], v[164:167], v[8:11]\n\t" \
  "ds_read_b64_tr_b16 v[130:131], %[b1e0] offset:45056\n\t" \
  "v_mfma_f32_16x16x32_bf16 v[12:15], v[172:175], v[168:171], v[12:15]\n\t" \
  "ds_read_b64_tr_b16 v[132:133], %[b1e1] offset:43008\n\t" \
  "v_mfma_f32_16x16x32_bf16 v[28:31], v[176:179], v[168:171], v[28:31]\n\t" \
  "ds_read_b128 v[112:115], %[a0] offset:16384\n\t" \
  "v_mfma_f32_16x16x32_bf16 v[24:27], v[176:179], v[164:167], v[24:27]\n\t" \
  "ds_read_b64_tr_b16 v[134:135], %[b1e1] offset:45056\n\t" \
  "v_mfma_f32_16x16x32_bf16 v[20:23], v[176:179], v[160:163], v[20:23]\n\t" \
  "ds_read_b64_tr_b16 v[136:137], %[b1e0] offset:47104\n\t" \
  "v_mfma_f32_16x16x32_bf16 v[16:19], v[176:179], v[156:159], v[16:19]\n\t" \
  "ds_read_b64_tr_b16 v[138:139], %[b1e0] offset:49152\n\t" \
  "v_mfma_f32_16x16x32_bf16 v[32:35], v[180:183], v[156:159], v[32:35]\n\t" \
  "ds_read_b128 v[116:119], %[a0] offset:17408\n\t" \
  "v_mfma_f32_16x16x32_bf16 v[36:39], v[180:183], v[160:163], v[36:39]\n\t" \
  "ds_read_b64_tr_b16 v[140:141], %[b1e1] offset:47104\n\t" \
  "v_mfma_f32_16x16x32_bf16 v[40:43], v[180:183], v[164:167], v[40:43]\n\t" \
  "ds_read_b64_tr_b16 v[142:143], %[b1e1] offset:49152\n\t" \
  "v_mfma_f32_16x16x32_bf16 v[44:47], v[180:183], v[168:171], v[44:47]\n\t" \
  "ds_read_b64_tr_b16 v[144:145], %[b1e0] offset:51200\n\t" \
  "v_mfma_f32_16x16x32_bf16 v[60:63], v[184:187], v[168:171], v[60:63]\n\t" \
  "ds_read_b128 v[120:123], %[a0] offset:18432\n\t" \
  "v_mfma_f32_16x16x32_bf16 v[56:59], v[184:187], v[164:167], v[56:59]\n\t" \
  "ds_read_b64_tr_b16 v[146:147], %[b1e0] offset:53248\n\t" \
  "v_mfma_f32_16x16x32_bf16 v[52:55], v[184:187], v[160:163], v[52:55]\n\t" \
  "ds_read_b64_tr_b16 v[148:149], %[b1e1] offset:51200\n\t" \
  "v_mfma_f32_16x16x32_bf16 v[48:51], v[184:187], v[156:159], v[48:51]\n\t" \
  "ds_read_b64_tr_b16 v[150:151], %[b1e1] offset:53248\n\t" \
  "v_mfma_f32_16x16x32_bf16 v[64:67], v[188:191], v[156:159], v[64:67]\n\t" \
  "ds_read_b128 v[124:127], %[a0] offset:19456\n\t" \
  "v_mfma_f32_16x16x32_bf16 v[68:71], v[188:191], v[160:163], v[68:71]\n\t" \
  "ds_read_b64_tr_b16 v[152:153], %[h1] offset:55296\n\t" \
  "v_mfma_f32_16x16x32_bf16 v[72:75], v[188:191], v[164:167], v[72:75]\n\t" \
  "ds_read_b64_tr_b16 v[154:155], %[h1] offset:56320\n\t" \
  "v_mfma_f32_16x16x32_bf16 v[76:79], v[188:191], v[168:171], v[76:79]\n\t" \
  "v_mfma_f32_16x16x32_bf16 v[92:95], v[192:195], v[168:171], v[92:95]\n\t" \
  "v_mfma_f32_16x16x32_bf16 v[88:91], v[192:195], v[164:167], v[88:91]\n\t" \
  "v_mfma_f32_16x16x32_bf16 v[84:87], v[192:195], v[160:163], v[84:87]\n\t" \
  "v_mfma_f32_16x16x32_bf16 v[80:83], v[192:195], v[156:159], v[80:83]\n\t" \
  "v_mfma_f32_16x16x32_bf16 v[96:99], v[196:199], v[156:159], v[96:99]\n\t" \
  "v_mfma_f32_16x16x32_bf16 v[100:103], v[196:199], v[160:163], v[100:103]\n\t" \
  "v_mfma_f32_16x16x32_bf16 v[104:107], v[196:199], v[164:167], v[104:107]\n\t" \
  "v_mfma_f32_16x16x32_bf16 v[108:111], v[196:199], v[168:171], v[108:111]\n\t" \
  "s_waitcnt lgkmcnt(0)\n\t" \
  "v_mfma_f32_16x16x32_bf16 v[0:3], v[128:131], v[112:115], v[0:3]\n\t" \
  "s_barrier\n\t" \
  "v_mfma_f32_16x16x32_bf16 v[4:7], v[128:131], v[116:119], v[4:7]\n\t" \
  "ds_read_b64_tr_b16 v[172:173], %[b1e0] offset:28672\n\t" \
  "v_mfma_f32_16x16x32_bf16 v[8:11], v[128:131], v[120:123], v[8:11]\n\t" \
  "ds_read_b64_tr_b16 v[174:175], %[b1e0] offset:30720\n\t" \
  "v_mfma_f32_16x16x32_bf16 v[12:15], v[128:131], v[124:127], v[12:15]\n\t" \
  "ds_read_b64_tr_b16 v[176:177], %[b1e1] offset:28672\n\t" \
  "v_mfma_f32_16x16x32_bf16 v[28:31], v[132:135], v[124:127], v[28:31]\n\t" \
  "ds_read_b128 v[156:159], %[a0] offset:24576\n\t" \
  "v_mfma_f32_16x16x32_bf16 v[24:27], v[132:135], v[120:123], v[24:27]\n\t" \
  "ds_read_b64_tr_b16 v[178:179], %[b1e1] offset:30720\n\t" \
  "v_mfma_f32_16x16x32_bf16 v[20:23], v[132:135], v[116:119], v[20:23]\n\t" \
  "ds_read_b64_tr_b16 v[180:181], %[b1e0] offset:32768\n\t" \
  "v_mfma_f32_16x16x32_bf16 v[16:19], v[132:135], v[112:115], v[16:19]\n\t" \
  "ds_read_b64_tr_b16 v[182:183], %[b1e0] offset:34816\n\t" \
  "v_mfma_f32_16x16x32_bf16 v[32:35], v[136:139], v[112:115], v[32:35]\n\t" \
  "ds_read_b128 v[160:163], %[a0] offset:25600\n\t" \
  "v_mfma_f32_16x16x32_bf16 v[36:39], v[136:139], v[116:119], v[36:39]\n\t" \
  "ds_read_b64_tr_b16 v[184:185], %[b1e1] offset:32768\n\t" \
  "v_mfma_f32_16x16x32_bf16 v[40:43], v[136:139], v[120:123], v[40:43]\n\t" \
  "ds_read_b64_tr_b16 v[186:187], %[b1e1] offset:34816\n\t" \
  "v_mfma_f32_16x16x32_bf16 v[44:47], v[136:139], v[124:127], v[44:47]\n\t" \
  "ds_read_b64_tr_b16 v[188:189], %[b1e0] offset:36864\n\t" \
  "v_mfma_f32_16x16x32_bf16 v[60:63], v[140:143], v[124:127], v[60:63]\n\t" \
  "ds_read_b128 v[164:167], %[a0] offset:26624\n\t" \
  "v_mfma_f32_16x16x32_bf16 v[56:59], v[140:143], v[120:123], v[56:59]\n\t" \
  "ds_read_b64_tr_b16 v[190:191], %[b1e0] offset:38912\n\t" \
  "v_mfma_f32_16x16x32_bf16 v[52:55], v[140:143], v[116:119], v[52:55]\n\t" \
  "ds_read_b64_tr_b16 v[192:193], %[b1e1] offset:36864\n\t" \
  "v_mfma_f32_16x16x32_bf16 v[48:51], v[140:143], v[112:115], v[48:51]\n\t" \
  "ds_read_b64_tr_b16 v[194:195], %[b1e1] offset:38912\n\t" \
  "v_mfma_f32_16x16x32_bf16 v[64:67], v[144:147], v[112:115], v[64:67]\n\t" \
  "ds_read_b128 v[168:171], %[a0] offset:27648\n\t" \
  "v_mfma_f32_16x16x32_bf16 v[68:71], v[144:147], v[116:119], v[68:71]\n\t" \
  "ds_read_b64_tr_b16 v[196:197], %[h1] offset:40960\n\t" \
  "v_mfma_f32_16x16x32_bf16 v[72:75], v[144:147], v[120:123], v[72:75]\n\t" \
  "ds_read_b64_tr_b16 v[198:199], %[h1] offset:41984\n\t" \
  "v_mfma_f32_16x16x32_bf16 v[76:79], v[144:147], v[124:127], v[76:79]\n\t" \
  "v_mfma_f32_16x16x32_bf16 v[92:95], v[148:151], v[124:127], v[92:95]\n\t" \
  "v_mfma_f32_16x16x32_bf16 v[88:91], v[148:151], v[120:123], v[88:91]\n\t" \
  "v_mfma_f32_16x16x32_bf16 v[84:87], v[148:151], v[116:119], v[84:87]\n\t" \
  "v_mfma_f32_16x16x32_bf16 v[80:83], v[148:151], v[112:115], v[80:83]\n\t" \
  "v_mfma_f32_16x16x32_bf16 v[96:99], v[152:155], v[112:115], v[96:99]\n\t" \
  "v_mfma_f32_16x16x32_bf16 v[100:103], v[152:155], v[116:119], v[100:103]\n\t" \
  "v_mfma_f32_16x16x32_bf16 v[104:107], v[152:155], v[120:123], v[104:107]\n\t" \
  "v_mfma_f32_16x16x32_bf16 v[108:111], v[152:155], v[124:127], v[108:111]\n\t" \
  "s_waitcnt lgkmcnt(0)\n\t" \
  "v_mfma_f32_16x16x32_bf16 v[0:3], v[172:175], v[156:159], v[0:3]\n\t" \
  "s_barrier\n\t" \
  "v_mfma_f32_16x16x32_bf16 v[4:7], v[172:175], v[160:163], v[4:7]\n\t" \
  "ds_read_b64_tr_b16 v[128:129], %[b1e0] offset:14336\n\t" \
  "v_mfma_f32_16x16x32_bf16 v[8:11], v[172:175], v[164:167], v[8:11]\n\t" \
  "ds_read_b64_tr_b16 v[130:131], %[b1e0] offset:16384\n\t" \
  "v_mfma_f32_16x16x32_bf16 v[12:15], v[172:175], v[168:171], v[12:15]\n\t" \
  "ds_read_b64_tr_b16 v[132:133], %[b1e1] offset:14336\n\t" \
  "v_mfma_f32_16x16x32_bf16 v[28:31], v[176:179], v[168:171], v[28:31]\n\t" \
  "ds_read_b128 v[112:115], %[a0] offset:0\n\t" \
  "v_mfma_f32_16x16x32_bf16 v[24:27], v[176:179], v[164:167], v[24:27]\n\t" \
  "ds_read_b64_tr_b16 v[134:135], %[b1e1] offset:16384\n\t" \
  "v_mfma_f32_16x16x32_bf16 v[20:23], v[176:179], v[160:163], v[20:23]\n\t" \
  "ds_read_b64_tr_b16 v[136:137], %[b1e0] offset:18432\n\t" \
  "v_mfma_f32_16x16x32_bf16 v[16:19], v[176:179], v[156:159], v[16:19]\n\t" \
  "ds_read_b64_tr_b16 v[138:139], %[b1e0] offset:20480\n\t" \
  "v_mfma_f32_16x16x32_bf16 v[32:35], v[180:183], v[156:159], v[32:35]\n\t" \
  "ds_read_b128 v[116:119], %[a0] offset:1024\n\t" \
  "v_mfma_f32_16x16x32_bf16 v[36:39], v[180:183], v[160:163], v[36:39]\n\t" \
  "ds_read_b64_tr_b16 v[140:141], %[b1e1] offset:18432\n\t" \
  "v_mfma_f32_16x16x32_bf16 v[40:43], v[180:183], v[164:167], v[40:43]\n\t" \
  "ds_read_b64_tr_b16 v[142:143], %[b1e1] offset:20480\n\t" \
  "v_mfma_f32_16x16x32_bf16 v[44:47], v[180:183], v[168:171], v[44:47]\n\t" \
  "ds_read_b64_tr_b16 v[144:145], %[b1e0] offset:22528\n\t" \
  "v_mfma_f32_16x16x32_bf16 v[60:63], v[184:187], v[168:171], v[60:63]\n\t" \
  "ds_read_b128 v[120:123], %[a0] offset:2048\n\t" \
  "v_mfma_f32_16x16x32_bf16 v[56:59], v[184:187], v[164:167], v[56:59]\n\t" \
  "ds_read_b64_tr_b16 v[146:147], %[b1e0] offset:24576\n\t" \
  "v_mfma_f32_16x16x32_bf16 v[52:55], v[184:187], v[160:163], v[52:55]\n\t" \
  "ds_read_b64_tr_b16 v[148:149], %[b1e1] offset:22528\n\t" \
  "v_mfma_f32_16x16x32_bf16 v[48:51], v[184:187], v[156:159], v[48:51]\n\t" \
  "ds_read_b64_tr_b16 v[150:151], %[b1e1] offset:24576\n\t" \
  "v_mfma_f32_16x16x32_bf16 v[64:67], v[188:191], v[156:159], v[64:67]\n\t" \
  "ds_read_b128 v[124:127], %[a0] offset:3072\n\t" \
  "v_mfma_f32_16x16x32_bf16 v[68:71], v[188:191], v[160:163], v[68:71]\n\t" \
  "ds_read_b64_tr_b16 v[152:153], %[h1] offset:26624\n\t" \
  "v_mfma_f32_16x16x32_bf16 v[72:75], v[188:191], v[164:167], v[72:75]\n\t" \
  "ds_read_b64_tr_b16 v[154:155], %[h1] offset:27648\n\t" \
  "v_mfma_f32_16x16x32_bf16 v[76:79], v[188:191], v[168:171], v[76:79]\n\t" \
  "v_mfma_f32_16x16x32_bf16 v[92:95], v[192:195], v[168:171], v[92:95]\n\t" \
  "v_mfma_f32_16x16x32_bf16 v[88:91], v[192:195], v[164:167], v[88:91]\n\t" \
  "v_mfma_f32_16x16x32_bf16 v[84:87], v[192:195], v[160:163], v[84:87]\n\t" \
  "v_mfma_f32_16x16x32_bf16 v[80:83], v[192:195], v[156:159], v[80:83]\n\t" \
  "v_mfma_f32_16x16x32_bf16 v[96:99], v[196:199], v[156:159], v[96:99]\n\t" \
  "v_mfma_f32_16x16x32_bf16 v[100:103], v[196:199], v[160:163], v[100:103]\n\t" \
  "v_mfma_f32_16x16x32_bf16 v[104:107], v[196:199], v[164:167], v[104:107]\n\t" \
  "v_mfma_f32_16x16x32_bf16 v[108:111], v[196:199], v[168:171], v[108:111]\n\t" \
  "s_waitcnt lgkmcnt(0)\n\t" \
  "v_mfma_f32_16x16x32_bf16 v[0:3], v[128:131], v[112:115], v[0:3]\n\t" \
  "s_barrier\n\t" \
  "v_mfma_f32_16x16x32_bf16 v[4:7], v[128:131], v[116:119], v[4:7]\n\t" \
  "ds_read_b64_tr_b16 v[172:173], %[b1e0] offset:0\n\t" \
  "v_mfma_f32_16x16x32_bf16 v[8:11], v[128:131], v[120:123], v[8:11]\n\t" \
  "ds_read_b64_tr_b16 v[174:175], %[b1e0] offset:2048\n\t" \
  "v_mfma_f32_16x16x32_bf16 v[12:15], v[128:131], v[124:127], v[12:15]\n\t" \
  "ds_read_b64_tr_b16 v[176:177], %[b1e1] offset:0\n\t" \
  "v_mfma_f32_16x16x32_bf16 v[28:31], v[132:135], v[124:127], v[28:31]\n\t" \
  "ds_read_b128 v[156:159], %[a0] offset:8192\n\t" \
  "v_mfma_f32_16x16x32_bf16 v[24:27], v[132:135], v[120:123], v[24:27]\n\t" \
  "ds_read_b64_tr_b16 v[178:179], %[b1e1] offset:2048\n\t" \
  "v_mfma_f32_16x16x32_bf16 v[20:23], v[132:135], v[116:119], v[20:23]\n\t" \
  "ds_read_b64_tr_b16 v[180:181], %[b1e0] offset:4096\n\t" \
  "v_mfma_f32_16x16x32_bf16 v[16:19], v[132:135], v[112:115], v[16:19]\n\t" \
  "ds_read_b64_tr_b16 v[182:183], %[b1e0] offset:6144\n\t" \
  "v_mfma_f32_16x16x32_bf16 v[32:35], v[136:139], v[112:115], v[32:35]\n\t" \
  "ds_read_b128 v[160:163], %[a0] offset:9216\n\t" \
  "v_mfma_f32_16x16x32_bf16 v[36:39], v[136:139], v[116:119], v[36:39]\n\t" \
  "ds_read_b64_tr_b16 v[184:185], %[b1e1] offset:4096\n\t" \
  "v_mfma_f32_16x16x32_bf16 v[40:43], v[136:139], v[120:123], v[40:43]\n\t" \
  "ds_read_b64_tr_b16 v[186:187], %[b1e1] offset:6144\n\t" \
  "v_mfma_f32_16x16x32_bf16 v[44:47], v[136:139], v[124:127], v[44:47]\n\t" \
  "ds_read_b64_tr_b16 v[188:189], %[b1e0] offset:8192\n\t" \
  "v_mfma_f32_16x16x32_bf16 v[60:63], v[140:143], v[124:127], v[60:63]\n\t" \
  "ds_read_b128 v[164:167], %[a0] offset:10240\n\t" \
  "v_mfma_f32_16x16x32_bf16 v[56:59], v[140:143], v[120:123], v[56:59]\n\t" \
  "ds_read_b64_tr_b16 v[190:191], %[b1e0] offset:10240\n\t" \
  "v_mfma_f32_16x16x32_bf16 v[52:55], v[140:143], v[116:119], v[52:55]\n\t" \
  "ds_read_b64_tr_b16 v[192:193], %[b1e1] offset:8192\n\t" \
  "v_mfma_f32_16x16x32_bf16 v[48:51], v[140:143], v[112:115], v[48:51]\n\t" \
  "ds_read_b64_tr_b16 v[194:195], %[b1e1] offset:10240\n\t" \
  "v_mfma_f32_16x16x32_bf16 v[64:67], v[144:147], v[112:115], v[64:67]\n\t" \
  "ds_read_b128 v[168:171], %[a0] offset:11264\n\t" \
  "v_mfma_f32_16x16x32_bf16 v[68:71], v[144:147], v[116:119], v[68:71]\n\t" \
  "ds_read_b64_tr_b16 v[196:197], %[h1] offset:12288\n\t" \
  "v_mfma_f32_16x16x32_bf16 v[72:75], v[144:147], v[120:123], v[72:75]\n\t" \
  "ds_read_b64_tr_b16 v[198:199], %[h1] offset:13312\n\t" \
  "v_mfma_f32_16x16x32_bf16 v[76:79], v[144:147], v[124:127], v[76:79]\n\t" \
  "v_mfma_f32_16x16x32_bf16 v[92:95], v[148:151], v[124:127], v[92:95]\n\t" \
  "v_mfma_f32_16x16x32_bf16 v[88:91], v[148:151], v[120:123], v[88:91]\n\t" \
  "v_mfma_f32_16x16x32_bf16 v[84:87], v[148:151], v[116:119], v[84:87]\n\t" \
  "v_mfma_f32_16x16x32_bf16 v[80:83], v[148:151], v[112:115], v[80:83]\n\t" \
  "v_mfma_f32_16x16x32_bf16 v[96:99], v[152:155], v[112:115], v[96:99]\n\t" \
  "v_mfma_f32_16x16x32_bf16 v[100:103], v[152:155], v[116:119], v[100:103]\n\t" \
  "v_mfma_f32_16x16x32_bf16 v[104:107], v[152:155], v[120:123], v[104:107]\n\t" \
  "v_mfma_f32_16x16x32_bf16 v[108:111], v[152:155], v[124:127], v[108:111]\n\t" \
  "s_waitcnt lgkmcnt(0)\n\t" \
  "v_mfma_f32_16x16x32_bf16 v[0:3], v[172:175], v[156:159], v[0:3]\n\t" \
  "s_barrier\n\t" \
  "v_mfma_f32_16x16x32_bf16 v[4:7], v[172:175], v[160:163], v[4:7]\n\t" \
  "ds_read_b64_tr_b16 v[128:129], %[b0e0] offset:43008\n\t" \
  "v_mfma_f32_16x16x32_bf16 v[8:11], v[172:175], v[164:167], v[8:11]\n\t" \
  "ds_read_b64_tr_b16 v[130:131], %[b0e0] offset:45056\n\t" \
  "v_mfma_f32_16x16x32_bf16 v[12:15], v[172:175], v[168:171], v[12:15]\n\t" \
  "ds_read_b64_tr_b16 v[132:133], %[b0e1] offset:43008\n\t" \
  "v_mfma_f32_16x16x32_bf16 v[28:31], v[176:179], v[168:171], v[28:31]\n\t" \
  "ds_read_b128 v[112:115], %[a0] offset:16384\n\t" \
  "v_mfma_f32_16x16x32_bf16 v[24:27], v[176:179], v[164:167], v[24:27]\n\t" \
  "ds_read_b64_tr_b16 v[134:135], %[b0e1] offset:45056\n\t" \
  "v_mfma_f32_16x16x32_bf16 v[20:23], v[176:179], v[160:163], v[20:23]\n\t" \
  "ds_read_b64_tr_b16 v[136:137], %[b0e0] offset:47104\n\t" \
  "v_mfma_f32_16x16x32_bf16 v[16:19], v[176:179], v[156:159], v[16:19]\n\t" \
  "ds_read_b64_tr_b16 v[138:139], %[b0e0] offset:49152\n\t" \
  "v_mfma_f32_16x16x32_bf16 v[32:35], v[180:183], v[156:159], v[32:35]\n\t" \
  "ds_read_b128 v[116:119], %[a0] offset:17408\n\t" \
  "v_mfma_f32_16x16x32_bf16 v[36:39], v[180:183], v[160:163], v[36:39]\n\t" \
  "ds_read_b64_tr_b16 v[140:141], %[b0e1] offset:47104\n\t" \
  "v_mfma_f32_16x16x32_bf16 v[40:43], v[180:183], v[164:167], v[40:43]\n\t" \
  "ds_read_b64_tr_b16 v[142:143], %[b0e1] offset:49152\n\t" \
  "v_mfma_f32_16x16x32_bf16 v[44:47], v[180:183], v[168:171], v[44:47]\n\t" \
  "ds_read_b64_tr_b16 v[144:145], %[b0e0] offset:51200\n\t" \
  "v_mfma_f32_16x16x32_bf16 v[60:63], v[184:187], v[168:171], v[60:63]\n\t" \
  "ds_read_b128 v[120:123], %[a0] offset:18432\n\t" \
  "v_mfma_f32_16x16x32_bf16 v[56:59], v[184:187], v[164:167], v[56:59]\n\t" \
  "ds_read_b64_tr_b16 v[146:147], %[b0e0] offset:53248\n\t" \
  "v_mfma_f32_16x16x32_bf16 v[52:55], v[184:187], v[160:163], v[52:55]\n\t" \
  "ds_read_b64_tr_b16 v[148:149], %[b0e1] offset:51200\n\t" \
  "v_mfma_f32_16x16x32_bf16 v[48:51], v[184:187], v[156:159], v[48:51]\n\t" \
  "ds_read_b64_tr_b16 v[150:151], %[b0e1] offset:53248\n\t" \
  "v_mfma_f32_16x16x32_bf16 v[64:67], v[188:191], v[156:159], v[64:67]\n\t" \
  "ds_read_b128 v[124:127], %[a0] offset:19456\n\t" \
  "v_mfma_f32_16x16x32_bf16 v[68:71], v[188:191], v[160:163], v[68:71]\n\t" \
  "ds_read_b64_tr_b16 v[152:153], %[h0] offset:55296\n\t" \
  "v_mfma_f32_16x16x32_bf16 v[72:75], v[188:191], v[164:167], v[72:75]\n\t" \
  "ds_read_b64_tr_b16 v[154:155], %[h0] offset:56320\n\t" \
  "v_mfma_f32_16x16x32_bf16 v[76:79], v[188:191], v[168:171], v[76:79]\n\t" \
  "v_mfma_f32_16x16x32_bf16 v[92:95], v[192:195], v[168:171], v[92:95]\n\t" \
  "v_mfma_f32_16x16x32_bf16 v[88:91], v[192:195], v[164:167], v[88:91]\n\t" \
  "v_mfma_f32_16x16x32_bf16 v[84:87], v[192:195], v[160:163], v[84:87]\n\t" \
  "v_mfma_f32_16x16x32_bf16 v[80:83], v[192:195], v[156:159], v[80:83]\n\t" \
  "v_mfma_f32_16x16x32_bf16 v[96:99], v[196:199], v[156:159], v[96:99]\n\t" \
  "v_mfma_f32_16x16x32_bf16 v[100:103], v[196:199], v[160:163], v[100:103]\n\t" \
  "v_mfma_f32_16x16x32_bf16 v[104:107], v[196:199], v[164:167], v[104:107]\n\t" \
  "v_mfma_f32_16x16x32_bf16 v[108:111], v[196:199], v[168:171], v[108:111]\n\t" \
  "s_waitcnt lgkmcnt(0)\n\t" \
  "v_mfma_f32_16x16x32_bf16 v[0:3], v[128:131], v[112:115], v[0:3]\n\t" \
  "s_barrier\n\t" \
  "v_mfma_f32_16x16x32_bf16 v[4:7], v[128:131], v[116:119], v[4:7]\n\t" \
  "ds_read_b64_tr_b16 v[172:173], %[b0e0] offset:28672\n\t" \
  "v_mfma_f32_16x16x32_bf16 v[8:11], v[128:131], v[120:123], v[8:11]\n\t" \
  "ds_read_b64_tr_b16 v[174:175], %[b0e0] offset:30720\n\t" \
  "v_mfma_f32_16x16x32_bf16 v[12:15], v[128:131], v[124:127], v[12:15]\n\t" \
  "ds_read_b64_tr_b16 v[176:177], %[b0e1] offset:28672\n\t" \
  "v_mfma_f32_16x16x32_bf16 v[28:31], v[132:135], v[124:127], v[28:31]\n\t" \
  "ds_read_b128 v[156:159], %[a0] offset:24576\n\t" \
  "v_mfma_f32_16x16x32_bf16 v[24:27], v[132:135], v[120:123], v[24:27]\n\t" \
  "ds_read_b64_tr_b16 v[178:179], %[b0e1] offset:30720\n\t" \
  "v_mfma_f32_16x16x32_bf16 v[20:23], v[132:135], v[116:119], v[20:23]\n\t" \
  "ds_read_b64_tr_b16 v[180:181], %[b0e0] offset:32768\n\t" \
  "v_mfma_f32_16x16x32_bf16 v[16:19], v[132:135], v[112:115], v[16:19]\n\t" \
  "ds_read_b64_tr_b16 v[182:183], %[b0e0] offset:34816\n\t" \
  "v_mfma_f32_16x16x32_bf16 v[32:35], v[136:139], v[112:115], v[32:35]\n\t" \
  "ds_read_b128 v[160:163], %[a0] offset:25600\n\t" \
  "v_mfma_f32_16x16x32_bf16 v[36:39], v[136:139], v[116:119], v[36:39]\n\t" \
  "ds_read_b64_tr_b16 v[184:185], %[b0e1] offset:32768\n\t" \
  "v_mfma_f32_16x16x32_bf16 v[40:43], v[136:139], v[120:123], v[40:43]\n\t" \
  "ds_read_b64_tr_b16 v[186:187], %[b0e1] offset:34816\n\t" \
  "v_mfma_f32_16x16x32_bf16 v[44:47], v[136:139], v[124:127], v[44:47]\n\t" \
  "ds_read_b64_tr_b16 v[188:189], %[b0e0] offset:36864\n\t" \
  "v_mfma_f32_16x16x32_bf16 v[60:63], v[140:143], v[124:127], v[60:63]\n\t" \
  "ds_read_b128 v[164:167], %[a0] offset:26624\n\t" \
  "v_mfma_f32_16x16x32_bf16 v[56:59], v[140:143], v[120:123], v[56:59]\n\t" \
  "ds_read_b64_tr_b16 v[190:191], %[b0e0] offset:38912\n\t" \
  "v_mfma_f32_16x16x32_bf16 v[52:55], v[140:143], v[116:119], v[52:55]\n\t" \
  "ds_read_b64_tr_b16 v[192:193], %[b0e1] offset:36864\n\t" \
  "v_mfma_f32_16x16x32_bf16 v[48:51], v[140:143], v[112:115], v[48:51]\n\t" \
  "ds_read_b64_tr_b16 v[194:195], %[b0e1] offset:38912\n\t" \
  "v_mfma_f32_16x16x32_bf16 v[64:67], v[144:147], v[112:115], v[64:67]\n\t" \
  "ds_read_b128 v[168:171], %[a0] offset:27648\n\t" \
  "v_mfma_f32_16x16x32_bf16 v[68:71], v[144:147], v[116:119], v[68:71]\n\t" \
  "ds_read_b64_tr_b16 v[196:197], %[h0] offset:40960\n\t" \
  "v_mfma_f32_16x16x32_bf16 v[72:75], v[144:147], v[120:123], v[72:75]\n\t" \
  "ds_read_b64_tr_b16 v[198:199], %[h0] offset:41984\n\t" \
  "v_mfma_f32_16x16x32_bf16 v[76:79], v[144:147], v[124:127], v[76:79]\n\t" \
  "v_mfma_f32_16x16x32_bf16 v[92:95], v[148:151], v[124:127], v[92:95]\n\t" \
  "v_mfma_f32_16x16x32_bf16 v[88:91], v[148:151], v[120:123], v[88:91]\n\t" \
  "v_mfma_f32_16x16x32_bf16 v[84:87], v[148:151], v[116:119], v[84:87]\n\t" \
  "v_mfma_f32_16x16x32_bf16 v[80:83], v[148:151], v[112:115], v[80:83]\n\t" \
  "v_mfma_f32_16x16x32_bf16 v[96:99], v[152:155], v[112:115], v[96:99]\n\t" \
  "v_mfma_f32_16x16x32_bf16 v[100:103], v[152:155], v[116:119], v[100:103]\n\t" \
  "v_mfma_f32_16x16x32_bf16 v[104:107], v[152:155], v[120:123], v[104:107]\n\t" \
  "v_mfma_f32_16x16x32_bf16 v[108:111], v[152:155], v[124:127], v[108:111]\n\t" \
  "s_waitcnt lgkmcnt(0)\n\t" \
  "v_mfma_f32_16x16x32_bf16 v[0:3], v[172:175], v[156:159], v[0:3]\n\t" \
  "s_barrier\n\t" \
  "v_mfma_f32_16x16x32_bf16 v[4:7], v[172:175], v[160:163], v[4:7]\n\t" \
  "ds_read_b64_tr_b16 v[128:129], %[b0e0] offset:14336\n\t" \
  "v_mfma_f32_16x16x32_bf16 v[8:11], v[172:175], v[164:167], v[8:11]\n\t" \
  "ds_read_b64_tr_b16 v[130:131], %[b0e0] offset:16384\n\t" \
  "v_mfma_f32_16x16x32_bf16 v[12:15], v[172:175], v[168:171], v[12:15]\n\t" \
  "ds_read_b64_tr_b16 v[132:133], %[b0e1] offset:14336\n\t" \
  "v_mfma_f32_16x16x32_bf16 v[28:31], v[176:179], v[168:171], v[28:31]\n\t" \
  "ds_read_b128 v[112:115], %[a0] offset:0\n\t" \
  "v_mfma_f32_16x16x32_bf16 v[24:27], v[176:179], v[164:167], v[24:27]\n\t" \
  "ds_read_b64_tr_b16 v[134:135], %[b0e1] offset:16384\n\t" \
  "v_mfma_f32_16x16x32_bf16 v[20:23], v[176:179], v[160:163], v[20:23]\n\t" \
  "ds_read_b64_tr_b16 v[136:137], %[b0e0] offset:18432\n\t" \
  "v_mfma_f32_16x16x32_bf16 v[16:19], v[176:179], v[156:159], v[16:19]\n\t" \
  "ds_read_b64_tr_b16 v[138:139], %[b0e0] offset:20480\n\t" \
  "v_mfma_f32_16x16x32_bf16 v[32:35], v[180:183], v[156:159], v[32:35]\n\t" \
  "ds_read_b128 v[116:119], %[a0] offset:1024\n\t" \
  "v_mfma_f32_16x16x32_bf16 v[36:39], v[180:183], v[160:163], v[36:39]\n\t" \
  "ds_read_b64_tr_b16 v[140:141], %[b0e1] offset:18432\n\t" \
  "v_mfma_f32_16x16x32_bf16 v[40:43], v[180:183], v[164:167], v[40:43]\n\t" \
  "ds_read_b64_tr_b16 v[142:143], %[b0e1] offset:20480\n\t" \
  "v_mfma_f32_16x16x32_bf16 v[44:47], v[180:183], v[168:171], v[44:47]\n\t" \
  "ds_read_b64_tr_b16 v[144:145], %[b0e0] offset:22528\n\t" \
  "v_mfma_f32_16x16x32_bf16 v[60:63], v[184:187], v[168:171], v[60:63]\n\t" \
  "ds_read_b128 v[120:123], %[a0] offset:2048\n\t" \
  "v_mfma_f32_16x16x32_bf16 v[56:59], v[184:187], v[164:167], v[56:59]\n\t" \
  "ds_read_b64_tr_b16 v[146:147], %[b0e0] offset:24576\n\t" \
  "v_mfma_f32_16x16x32_bf16 v[52:55], v[184:187], v[160:163], v[52:55]\n\t" \
  "ds_read_b64_tr_b16 v[148:149], %[b0e1] offset:22528\n\t" \
  "v_mfma_f32_16x16x32_bf16 v[48:51], v[184:187], v[156:159], v[48:51]\n\t" \
  "ds_read_b64_tr_b16 v[150:151], %[b0e1] offset:24576\n\t" \
  "v_mfma_f32_16x16x32_bf16 v[64:67], v[188:191], v[156:159], v[64:67]\n\t" \
  "ds_read_b128 v[124:127], %[a0] offset:3072\n\t" \
  "v_mfma_f32_16x16x32_bf16 v[68:71], v[188:191], v[160:163], v[68:71]\n\t" \
  "ds_read_b64_tr_b16 v[152:153], %[h0] offset:26624\n\t" \
  "v_mfma_f32_16x16x32_bf16 v[72:75], v[188:191], v[164:167], v[72:75]\n\t" \
  "ds_read_b64_tr_b16 v[154:155], %[h0] offset:27648\n\t" \
  "v_mfma_f32_16x16x32_bf16 v[76:79], v[188:191], v[168:171], v[76:79]\n\t" \
  "s_add_i32 %[cnt], %[cnt], -1\n\t" \
  "v_mfma_f32_16x16x32_bf16 v[92:95], v[192:195], v[168:171], v[92:95]\n\t" \
  "s_cmp_lg_u32 %[cnt], 0\n\t" \
  "v_mfma_f32_16x16x32_bf16 v[88:91], v[192:195], v[164:167], v[88:91]\n\t" \
  "v_mfma_f32_16x16x32_bf16 v[84:87], v[192:195], v[160:163], v[84:87]\n\t" \
  "v_mfma_f32_16x16x32_bf16 v[80:83], v[192:195], v[156:159], v[80:83]\n\t" \
  "v_mfma_f32_16x16x32_bf16 v[96:99], v[196:199], v[156:159], v[96:99]\n\t" \
  "v_mfma_f32_16x16x32_bf16 v[100:103], v[196:199], v[160:163], v[100:103]\n\t" \
  "v_mfma_f32_16x16x32_bf16 v[104:107], v[196:199], v[164:167], v[104:107]\n\t" \
  "v_mfma_f32_16x16x32_bf16 v[108:111], v[196:199], v[168:171], v[108:111]\n\t" \
  "s_cbranch_scc1 1b\n\t" \
  "s_waitcnt lgkmcnt(0)\n\t" \
  "s_nop 7\n\t" \
  ""
#define FF1_CLOB "v112","v113","v114","v115","v116","v117","v118","v119","v120","v121","v122","v123","v124","v125","v126","v127","v128","v129","v130","v131","v132","v133","v134","v135","v136","v137","v138","v139","v140","v141","v142","v143","v144","v145","v146","v147","v148","v149","v150","v151","v152","v153","v154","v155","v156","v157","v158","v159","v160","v161","v162","v163","v164","v165","v166","v167","v168","v169","v170","v171","v172","v173","v174","v175","v176","v177","v178","v179","v180","v181","v182","v183","v184","v185","v186","v187","v188","v189","v190","v191","v192","v193","v194","v195","v196","v197","v198","v199"
#define FF2_ASM \
  "s_mov_b32 %[cnt], 3\n\t" \
  "s_barrier\n\t" \
  "ds_read_b64_tr_b16 v[144:145], %[b0e0] offset:49152\n\t" \
  "ds_read_b64_tr_b16 v[146:147], %[b0e0] offset:51200\n\t" \
  "ds_read_b64_tr_b16 v[148:149], %[b0e1] offset:49152\n\t" \
  "ds_read_b128 v[128:131], %[a0] offset:32768\n\t" \
  "ds_read_b64_tr_b16 v[150:151], %[b0e1] offset:51200\n\t" \
  "ds_read_b64_tr_b16 v[152:153], %[b0e0] offset:53248\n\t" \
  "ds_read_b64_tr_b16 v[154:155], %[b0e0] offset:55296\n\t" \
  "ds_read_b128 v[132:135], %[a0] offset:33792\n\t" \
  "ds_read_b64_tr_b16 v[156:157], %[b0e1] offset:53248\n\t" \
  "ds_read_b64_tr_b16 v[158:159], %[b0e1] offset:55296\n\t" \
  "ds_read_b64_tr_b16 v[160:161], %[b0e0] offset:57344\n\t" \
  "ds_read_b128 v[136:139], %[a0] offset:34816\n\t" \
  "ds_read_b64_tr_b16 v[162:163], %[b0e0] offset:59392\n\t" \
  "ds_read_b64_tr_b16 v[164:165], %[b0e1] offset:57344\n\t" \
  "ds_read_b64_tr_b16 v[166:167], %[b0e1] offset:59392\n\t" \
  "ds_read_b128 v[140:143], %[a0] offset:35840\n\t" \
  "ds_read_b64_tr_b16 v[168:169], %[b0e0] offset:61440\n\t" \
  "ds_read_b64_tr_b16 v[170:171], %[b0e0] offset:63488\n\t" \
  "ds_read_b64_tr_b16 v[172:173], %[b0e1] offset:61440\n\t" \
  "ds_read_b64_tr_b16 v[174:175], %[b0e1] offset:63488\n\t" \
  "s_waitcnt lgkmcnt(0)\n\t" \
  "v_mfma_f32_16x16x32_bf16 v[0:3], v[144:147], v[128:131], 0\n\t" \
  "s_barrier\n\t" \
  "v_mfma_f32_16x16x32_bf16 v[4:7], v[144:147], v[132:135], 0\n\t" \
  "ds_read_b64_tr_b16 v[192:193], %[b0e0] offset:32768\n\t" \
  "v_mfma_f32_16x16x32_bf16 v[8:11], v[144:147], v[136:139], 0\n\t" \
  "ds_read_b64_tr_b16 v[194:195], %[b0e0] offset:34816\n\t" \
  "v_mfma_f32_16x16x32_bf16 v[12:15], v[144:147], v[140:143], 0\n\t" \
  "ds_read_b64_tr_b16 v[196:197], %[b0e1] offset:32768\n\t" \
  "v_mfma_f32_16x16x32_bf16 v[28:31], v[148:151], v[140:143], 0\n\t" \
  "ds_read_b128 v[176:179], %[a0] offset:40960\n\t" \
  "v_mfma_f32_16x16x32_bf16 v[24:27], v[148:151], v[136:139], 0\n\t" \
  "ds_read_b64_tr_b16 v[198:199], %[b0e1] offset:34816\n\t" \
  "v_mfma_f32_16x16x32_bf16 v[20:23], v[148:151], v[132:135], 0\n\t" \
  "ds_read_b64_tr_b16 v[200:201], %[b0e0] offset:36864\n\t" \
  "v_mfma_f32_16x16x32_bf16 v[16:19], v[148:151], v[128:131], 0\n\t" \
  "ds_read_b64_tr_b16 v[202:203], %[b0e0] offset:38912\n\t" \
  "v_mfma_f32_16x16x32_bf16 v[32:35], v[152:155], v[128:131], 0\n\t" \
  "ds_read_b128 v[180:183], %[a0] offset:41984\n\t" \
  "v_mfma_f32_16x16x32_bf16 v[36:39], v[152:155], v[132:135], 0\n\t" \
  "ds_read_b64_tr_b16 v[204:205], %[b0e1] offset:36864\n\t" \
  "v_mfma_f32_16x16x32_bf16 v[40:43], v[152:155], v[136:139], 0\n\t" \
  "ds_read_b64_tr_b16 v[206:207], %[b0e1] offset:38912\n\t" \
  "v_mfma_f32_16x16x32_bf16 v[44:47], v[152:155], v[140:143], 0\n\t" \
  "ds_read_b64_tr_b16 v[208:209], %[b0e0] offset:40960\n\t" \
  "v_mfma_f32_16x16x32_bf16 v[60:63], v[156:159], v[140:143], 0\n\t" \
  "ds_read_b128 v[184:187], %[a0] offset:43008\n\t" \
  "v_mfma_f32_16x16x32_bf16 v[56:59], v[156:159], v[136:139], 0\n\t" \
  "ds_read_b64_tr_b16 v[210:211], %[b0e0] offset:43008\n\t" \
  "v_mfma_f32_16x16x32_bf16 v[52:55], v[156:159], v[132:135], 0\n\t" \
  "ds_read_b64_tr_b16 v[212:213], %[b0e1] offset:40960\n\t" \
  "v_mfma_f32_16x16x32_bf16 v[48:51], v[156:159], v[128:131], 0\n\t" \
  "ds_read_b64_tr_b16 v[214:215], %[b0e1] offset:43008\n\t" \
  "v_mfma_f32_16x16x32_bf16 v[64:67], v[160:163], v[128:131], 0\n\t" \
  "ds_read_b128 v[188:191], %[a0] offset:44032\n\t" \
  "v_mfma_f32_16x16x32_bf16 v[68:71], v[160:163], v[132:135], 0\n\t" \
  "ds_read_b64_tr_b16 v[216:217], %[b0e0] offset:45056\n\t" \
  "v_mfma_f32_16x16x32_bf16 v[72:75], v[160:163], v[136:139], 0\n\t" \
  "ds_read_b64_tr_b16 v[218:219], %[b0e0] offset:47104\n\t" \
  "v_mfma_f32_16x16x32_bf16 v[76:79], v[160:163], v[140:143], 0\n\t" \
  "ds_read_b64_tr_b16 v[220:221], %[b0e1] offset:45056\n\t" \
  "v_mfma_f32_16x16x32_bf16 v[92:95], v[164:167], v[140:143], 0\n\t" \
  "ds_read_b64_tr_b16 v[222:223], %[b0e1] offset:47104\n\t" \
  "v_mfma_f32_16x16x32_bf16 v[88:91], v[164:167], v[136:139], 0\n\t" \
  "v_mfma_f32_16x16x32_bf16 v[84:87], v[164:167], v[132:135], 0\n\t" \
  "v_mfma_f32_16x16x32_bf16 v[80:83], v[164:167], v[128:131], 0\n\t" \
  "v_mfma_f32_16x16x32_bf16 v[96:99], v[168:171], v[128:131], 0\n\t" \
  "v_mfma_f32_16x16x32_bf16 v[100:103], v[168:171], v[132:135], 0\n\t" \
  "v_mfma_f32_16x16x32_bf16 v[104:107], v[168:171], v[136:139], 0\n\t" \
  "v_mfma_f32_16x16x32_bf16 v[108:111], v[168:171], v[140:143], 0\n\t" \
  "v_mfma_f32_16x16x32_bf16 v[124:127], v[172:175], v[140:143], 0\n\t" \
  "v_mfma_f32_16x16x32_bf16 v[120:123], v[172:175], v[136:139], 0\n\t" \
  "v_mfma_f32_16x16x32_bf16 v[116:119], v[172:175], v[132:135], 0\n\t" \
  "v_mfma_f32_16x16x32_bf16 v[112:115], v[172:175], v[128:131], 0\n\t" \
  "s_branch 2f\n\t" \
  "1:\n\t" \
  "s_waitcnt lgkmcnt(0)\n\t" \
  "v_mfma_f32_16x16x32_bf16 v[0:3], v[144:147], v[128:131], v[0:3]\n\t" \
  "s_barrier\n\t" \
  "v_mfma_f32_16x16x32_bf16 v[4:7], v[144:147], v[132:135], v[4:7]\n\t" \
  "ds_read_b64_tr_b16 v[192:193], %[b0e0] offset:32768\n\t" \
  "v_mfma_f32_16x16x32_bf16 v[8:11], v[144:147], v[136:139], v[8:11]\n\t" \
  "ds_read_b64_tr_b16 v[194:195], %[b0e0] offset:34816\n\t" \
  "v_mfma_f32_16x16x32_bf16 v[12:15], v[144:147], v[140:143], v[12:15]\n\t" \
  "ds_read_b64_tr_b16 v[196:197], %[b0e1] offset:32768\n\t" \
  "v_mfma_f32_16x16x32_bf16 v[28:31], v[148:151], v[140:143], v[28:31]\n\t" \
  "ds_read_b128 v[176:179], %[a0] offset:8192\n\t" \
  "v_mfma_f32_16x16x32_bf16 v[24:27], v[148:151], v[136:139], v[24:27]\n\t" \
  "ds_read_b64_tr_b16 v[198:199], %[b0e1] offset:34816\n\t" \
  "v_mfma_f32_16x16x32_bf16 v[20:23], v[148:151], v[132:135], v[20:23]\n\t" \
  "ds_read_b64_tr_b16 v[200:201], %[b0e0] offset:36864\n\t" \
  "v_mfma_f32_16x16x32_bf16 v[16:19], v[148:151], v[128:131], v[16:19]\n\t" \
  "ds_read_b64_tr_b16 v[202:203], %[b0e0] offset:38912\n\t" \
  "v_mfma_f32_16x16x32_bf16 v[32:35], v[152:155], v[128:131], v[32:35]\n\t" \
  "ds_read_b128 v[180:183], %[a0] offset:9216\n\t" \
  "v_mfma_f32_16x16x32_bf16 v[36:39], v[152:155], v[132:135], v[36:39]\n\t" \
  "ds_read_b64_tr_b16 v[204:205], %[b0e1] offset:36864\n\t" \
  "v_mfma_f32_16x16x32_bf16 v[40:43], v[152:155], v[136:139], v[40:43]\n\t" \
  "ds_read_b64_tr_b16 v[206:207], %[b0e1] offset:38912\n\t" \
  "v_mfma_f32_16x16x32_bf16 v[44:47], v[152:155], v[140:143], v[44:47]\n\t" \
  "ds_read_b64_tr_b16 v[208:209], %[b0e0] offset:40960\n\t" \
  "v_mfma_f32_16x16x32_bf16 v[60:63], v[156:159], v[140:143], v[60:63]\n\t" \
  "ds_read_b128 v[184:187], %[a0] offset:10240\n\t" \
  "v_mfma_f32_16x16x32_bf16 v[56:59], v[156:159], v[136:139], v[56:59]\n\t" \
  "ds_read_b64_tr_b16 v[210:211], %[b0e0] offset:43008\n\t" \
  "v_mfma_f32_16x16x32_bf16 v[52:55], v[156:159], v[132:135], v[52:55]\n\t" \
  "ds_read_b64_tr_b16 v[212:213], %[b0e1] offset:40960\n\t" \
  "v_mfma_f32_16x16x32_bf16 v[48:51], v[156:159], v[128:131], v[48:51]\n\t" \
  "ds_read_b64_tr_b16 v[214:215], %[b0e1] offset:43008\n\t" \
  "v_mfma_f32_16x16x32_bf16 v[64:67], v[160:163], v[128:131], v[64:67]\n\t" \
  "ds_read_b128 v[188:191], %[a0] offset:11264\n\t" \
  "v_mfma_f32_16x16x32_bf16 v[68:71], v[160:163], v[132:135], v[68:71]\n\t" \
  "ds_read_b64_tr_b16 v[216:217], %[b0e0] offset:45056\n\t" \
  "v_mfma_f32_16x16x32_bf16 v[72:75], v[160:163], v[136:139], v[72:75]\n\t" \
  "ds_read_b64_tr_b16 v[218:219], %[b0e0] offset:47104\n\t" \
  "v_mfma_f32_16x16x32_bf16 v[76:79], v[160:163], v[140:143], v[76:79]\n\t" \
  "ds_read_b64_tr_b16 v[220:221], %[b0e1] offset:45056\n\t" \
  "v_mfma_f32_16x16x32_bf16 v[92:95], v[164:167], v[140:143], v[92:95]\n\t" \
  "ds_read_b64_tr_b16 v[222:223], %[b0e1] offset:47104\n\t" \
  "v_mfma_f32_16x16x32_bf16 v[88:91], v[164:167], v[136:139], v[88:91]\n\t" \
  "v_mfma_f32_16x16x32_bf16 v[84:87], v[164:167], v[132:135], v[84:87]\n\t" \
  "v_mfma_f32_16x16x32_bf16 v[80:83], v[164:167], v[128:131], v[80:83]\n\t" \
  "v_mfma_f32_16x16x32_bf16 v[96:99], v[168:171], v[128:131], v[96:99]\n\t" \
  "v_mfma_f32_16x16x32_bf16 v[100:103], v[168:171], v[132:135], v[100:103]\n\t" \
  "v_mfma_f32_16x16x32_bf16 v[104:107], v[168:171], v[136:139], v[104:107]\n\t" \
  "v_mfma_f32_16x16x32_bf16 v[108:111], v[168:171], v[140:143], v[108:111]\n\t" \
  "v_mfma_f32_16x16x32_bf16 v[124:127], v[172:175], v[140:143], v[124:127]\n\t" \
  "v_mfma_f32_16x16x32_bf16 v[120:123], v[172:175], v[136:139], v[120:123]\n\t" \
  "v_mfma_f32_16x16x32_bf16 v[116:119], v[172:175], v[132:135], v[116:119]\n\t" \
  "v_mfma_f32_16x16x32_bf16 v[112:115], v[172:175], v[128:131], v[112:115]\n\t" \
  "s_waitcnt lgkmcnt(0)\n\t" \
  "v_mfma_f32_16x16x32_bf16 v[0:3], v[192:195], v[176:179], v[0:3]\n\t" \
  "s_barrier\n\t" \
  "v_mfma_f32_16x16x32_bf16 v[4:7], v[192:195], v[180:183], v[4:7]\n\t" \
  "ds_read_b64_tr_b16 v[144:145], %[b0e0] offset:16384\n\t" \
  "v_mfma_f32_16x16x32_bf16 v[8:11], v[192:195], v[184:187], v[8:11]\n\t" \
  "ds_read_b64_tr_b16 v[146:147], %[b0e0] offset:18432\n\t" \
  "v_mfma_f32_16x16x32_bf16 v[12:15], v[192:195], v[188:191], v[12:15]\n\t" \
  "ds_read_b64_tr_b16 v[148:149], %[b0e1] offset:16384\n\t" \
  "v_mfma_f32_16x16x32_bf16 v[28:31], v[196:199], v[188:191], v[28:31]\n\t" \
  "ds_read_b128 v[128:131], %[a0] offset:16384\n\t" \
  "v_mfma_f32_16x16x32_bf16 v[24:27], v[196:199], v[184:187], v[24:27]\n\t" \
  "ds_read_b64_tr_b16 v[150:151], %[b0e1] offset:18432\n\t" \
  "v_mfma_f32_16x16x32_bf16 v[20:23], v[196:199], v[180:183], v[20:23]\n\t" \
  "ds_read_b64_tr_b16 v[152:153], %[b0e0] offset:20480\n\t" \
  "v_mfma_f32_16x16x32_bf16 v[16:19], v[196:199], v[176:179], v[16:19]\n\t" \
  "ds_read_b64_tr_b16 v[154:155], %[b0e0] offset:22528\n\t" \
  "v_mfma_f32_16x16x32_bf16 v[32:35], v[200:203], v[176:179], v[32:35]\n\t" \
  "ds_read_b128 v[132:135], %[a0] offset:17408\n\t" \
  "v_mfma_f32_16x16x32_bf16 v[36:39], v[200:203], v[180:183], v[36:39]\n\t" \
  "ds_read_b64_tr_b16 v[156:157], %[b0e1] offset:20480\n\t" \
  "v_mfma_f32_16x16x32_bf16 v[40:43], v[200:203], v[184:187], v[40:43]\n\t" \
  "ds_read_b64_tr_b16 v[158:159], %[b0e1] offset:22528\n\t" \
  "v_mfma_f32_16x16x32_bf16 v[44:47], v[200:203], v[188:191], v[44:47]\n\t" \
  "ds_read_b64_tr_b16 v[160:161], %[b0e0] offset:24576\n\t" \
  "v_mfma_f32_16x16x32_bf16 v[60:63], v[204:207], v[188:191], v[60:63]\n\t" \
  "ds_read_b128 v[136:139], %[a0] offset:18432\n\t" \
  "v_mfma_f32_16x16x32_bf16 v[56:59], v[204:207], v[184:187], v[56:59]\n\t" \
  "ds_read_b64_tr_b16 v[162:163], %[b0e0] offset:26624\n\t" \
  "v_mfma_f32_16x16x32_bf16 v[52:55], v[204:207], v[180:183], v[52:55]\n\t" \
  "ds_read_b64_tr_b16 v[164:165], %[b0e1] offset:24576\n\t" \
  "v_mfma_f32_16x16x32_bf16 v[48:51], v[204:207], v[176:179], v[48:51]\n\t" \
  "ds_read_b64_tr_b16 v[166:167], %[b0e1] offset:26624\n\t" \
  "v_mfma_f32_16x16x32_bf16 v[64:67], v[208:211], v[176:179], v[64:67]\n\t" \
  "ds_read_b128 v[140:143], %[a0] offset:19456\n\t" \
  "v_mfma_f32_16x16x32_bf16 v[68:71], v[208:211], v[180:183], v[68:71]\n\t" \
  "ds_read_b64_tr_b16 v[168:169], %[b0e0] offset:28672\n\t" \
  "v_mfma_f32_16x16x32_bf16 v[72:75], v[208:211], v[184:187], v[72:75]\n\t" \
  "ds_read_b64_tr_b16 v[170:171], %[b0e0] offset:30720\n\t" \
  "v_mfma_f32_16x16x32_bf16 v[76:79], v[208:211], v[188:191], v[76:79]\n\t" \
  "ds_read_b64_tr_b16 v[172:173], %[b0e1] offset:28672\n\t" \
  "v_mfma_f32_16x16x32_bf16 v[92:95], v[212:215], v[188:191], v[92:95]\n\t" \
  "ds_read_b64_tr_b16 v[174:175], %[b0e1] offset:30720\n\t" \
  "v_mfma_f32_16x16x32_bf16 v[88:91], v[212:215], v[184:187], v[88:91]\n\t" \
  "v_mfma_f32_16x16x32_bf16 v[84:87], v[212:215], v[180:183], v[84:87]\n\t" \
  "v_mfma_f32_16x16x32_bf16 v[80:83], v[212:215], v[176:179], v[80:83]\n\t" \
  "v_mfma_f32_16x16x32_bf16 v[96:99], v[216:219], v[176:179], v[96:99]\n\t" \
  "v_mfma_f32_16x16x32_bf16 v[100:103], v[216:219], v[180:183], v[100:103]\n\t" \
  "v_mfma_f32_16x16x32_bf16 v[104:107], v[216:219], v[184:187], v[104:107]\n\t" \
  "v_mfma_f32_16x16x32_bf16 v[108:111], v[216:219], v[188:191], v[108:111]\n\t" \
  "v_mfma_f32_16x16x32_bf16 v[124:127], v[220:223], v[188:191], v[124:127]\n\t" \
  "v_mfma_f32_16x16x32_bf16 v[120:123], v[220:223], v[184:187], v[120:123]\n\t" \
  "v_mfma_f32_16x16x32_bf16 v[116:119], v[220:223], v[180:183], v[116:119]\n\t" \
  "v_mfma_f32_16x16x32_bf16 v[112:115], v[220:223], v[176:179], v[112:115]\n\t" \
  "s_waitcnt lgkmcnt(0)\n\t" \
  "v_mfma_f32_16x16x32_bf16 v[0:3], v[144:147], v[128:131], v[0:3]\n\t" \
  "s_barrier\n\t" \
  "v_mfma_f32_16x16x32_bf16 v[4:7], v[144:147], v[132:135], v[4:7]\n\t" \
  "ds_read_b64_tr_b16 v[192:193], %[b0e0] offset:0\n\t" \
  "v_mfma_f32_16x16x32_bf16 v[8:11], v[144:147], v[136:139], v[8:11]\n\t" \
  "ds_read_b64_tr_b16 v[194:195], %[b0e0] offset:2048\n\t" \
  "v_mfma_f32_16x16x32_bf16 v[12:15], v[144:147], v[140:143], v[12:15]\n\t" \
  "ds_read_b64_tr_b16 v[196:197], %[b0e1] offset:0\n\t" \
  "v_mfma_f32_16x16x32_bf16 v[28:31], v[148:151], v[140:143], v[28:31]\n\t" \
  "ds_read_b128 v[176:179], %[a0] offset:24576\n\t" \
  "v_mfma_f32_16x16x32_bf16 v[24:27], v[148:151], v[136:139], v[24:27]\n\t" \
  "ds_read_b64_tr_b16 v[198:199], %[b0e1] offset:2048\n\t" \
  "v_mfma_f32_16x16x32_bf16 v[20:23], v[148:151], v[132:135], v[20:23]\n\t" \
  "ds_read_b64_tr_b16 v[200:201], %[b0e0] offset:4096\n\t" \
  "v_mfma_f32_16x16x32_bf16 v[16:19], v[148:151], v[128:131], v[16:19]\n\t" \
  "ds_read_b64_tr_b16 v[202:203], %[b0e0] offset:6144\n\t" \
  "v_mfma_f32_16x16x32_bf16 v[32:35], v[152:155], v[128:131], v[32:35]\n\t" \
  "ds_read_b128 v[180:183], %[a0] offset:25600\n\t" \
  "v_mfma_f32_16x16x32_bf16 v[36:39], v[152:155], v[132:135], v[36:39]\n\t" \
  "ds_read_b64_tr_b16 v[204:205], %[b0e1] offset:4096\n\t" \
  "v_mfma_f32_16x16x32_bf16 v[40:43], v[152:155], v[136:139], v[40:43]\n\t" \
  "ds_read_b64_tr_b16 v[206:207], %[b0e1] offset:6144\n\t" \
  "v_mfma_f32_16x16x32_bf16 v[44:47], v[152:155], v[140:143], v[44:47]\n\t" \
  "ds_read_b64_tr_b16 v[208:209], %[b0e0] offset:8192\n\t" \
  "v_mfma_f32_16x16x32_bf16 v[60:63], v[156:159], v[140:143], v[60:63]\n\t" \
  "ds_read_b128 v[184:187], %[a0] offset:26624\n\t" \
  "v_mfma_f32_16x16x32_bf16 v[56:59], v[156:159], v[136:139], v[56:59]\n\t" \
  "ds_read_b64_tr_b16 v[210:211], %[b0e0] offset:10240\n\t" \
  "v_mfma_f32_16x16x32_bf16 v[52:55], v[156:159], v[132:135], v[52:55]\n\t" \
  "ds_read_b64_tr_b16 v[212:213], %[b0e1] offset:8192\n\t" \
  "v_mfma_f32_16x16x32_bf16 v[48:51], v[156:159], v[128:131], v[48:51]\n\t" \
  "ds_read_b64_tr_b16 v[214:215], %[b0e1] offset:10240\n\t" \
  "v_mfma_f32_16x16x32_bf16 v[64:67], v[160:163], v[128:131], v[64:67]\n\t" \
  "ds_read_b128 v[188:191], %[a0] offset:27648\n\t" \
  "v_mfma_f32_16x16x32_bf16 v[68:71], v[160:163], v[132:135], v[68:71]\n\t" \
  "ds_read_b64_tr_b16 v[216:217], %[b0e0] offset:12288\n\t" \
  "v_mfma_f32_16x16x32_bf16 v[72:75], v[160:163], v[136:139], v[72:75]\n\t" \
  "ds_read_b64_tr_b16 v[218:219], %[b0e0] offset:14336\n\t" \
  "v_mfma_f32_16x16x32_bf16 v[76:79], v[160:163], v[140:143], v[76:79]\n\t" \
  "ds_read_b64_tr_b16 v[220:221], %[b0e1] offset:12288\n\t" \
  "v_mfma_f32_16x16x32_bf16 v[92:95], v[164:167], v[140:143], v[92:95]\n\t" \
  "ds_read_b64_tr_b16 v[222:223], %[b0e1] offset:14336\n\t" \
  "v_mfma_f32_16x16x32_bf16 v[88:91], v[164:167], v[136:139], v[88:91]\n\t" \
  "v_mfma_f32_16x16x32_bf16 v[84:87], v[164:167], v[132:135], v[84:87]\n\t" \
  "v_mfma_f32_16x16x32_bf16 v[80:83], v[164:167], v[128:131], v[80:83]\n\t" \
  "v_mfma_f32_16x16x32_bf16 v[96:99], v[168:171], v[128:131], v[96:99]\n\t" \
  "v_mfma_f32_16x16x32_bf16 v[100:103], v[168:171], v[132:135], v[100:103]\n\t" \
  "v_mfma_f32_16x16x32_bf16 v[104:107], v[168:171], v[136:139], v[104:107]\n\t" \
  "v_mfma_f32_16x16x32_bf16 v[108:111], v[168:171], v[140:143], v[108:111]\n\t" \
  "v_mfma_f32_16x16x32_bf16 v[124:127], v[172:175], v[140:143], v[124:127]\n\t" \
  "v_mfma_f32_16x16x32_bf16 v[120:123], v[172:175], v[136:139], v[120:123]\n\t" \
  "v_mfma_f32_16x16x32_bf16 v[116:119], v[172:175], v[132:135], v[116:119]\n\t" \
  "v_mfma_f32_16x16x32_bf16 v[112:115], v[172:175], v[128:131], v[112:115]\n\t" \
  "s_waitcnt lgkmcnt(0)\n\t" \
  "v_mfma_f32_16x16x32_bf16 v[0:3], v[192:195], v[176:179], v[0:3]\n\t" \
  "s_barrier\n\t" \
  "v_mfma_f32_16x16x32_bf16 v[4:7], v[192:195], v[180:183], v[4:7]\n\t" \
  "ds_read_b64_tr_b16 v[144:145], %[b0e0] offset:49152\n\t" \
  "v_mfma_f32_16x16x32_bf16 v[8:11], v[192:195], v[184:187], v[8:11]\n\t" \
  "ds_read_b64_tr_b16 v[146:147], %[b0e0] offset:51200\n\t" \
  "v_mfma_f32_16x16x32_bf16 v[12:15], v[192:195], v[188:191], v[12:15]\n\t" \
  "ds_read_b64_tr_b16 v[148:149], %[b0e1] offset:49152\n\t" \
  "v_mfma_f32_16x16x32_bf16 v[28:31], v[196:199], v[188:191], v[28:31]\n\t" \
  "ds_read_b128 v[128:131], %[a0] offset:32768\n\t" \
  "v_mfma_f32_16x16x32_bf16 v[24:27], v[196:199], v[184:187], v[24:27]\n\t" \
  "ds_read_b64_tr_b16 v[150:151], %[b0e1] offset:51200\n\t" \
  "v_mfma_f32_16x16x32_bf16 v[20:23], v[196:199], v[180:183], v[20:23]\n\t" \
  "ds_read_b64_tr_b16 v[152:153], %[b0e0] offset:53248\n\t" \
  "v_mfma_f32_16x16x32_bf16 v[16:19], v[196:199], v[176:179], v[16:19]\n\t" \
  "ds_read_b64_tr_b16 v[154:155], %[b0e0] offset:55296\n\t" \
  "v_mfma_f32_16x16x32_bf16 v[32:35], v[200:203], v[176:179], v[32:35]\n\t" \
  "ds_read_b128 v[132:135], %[a0] offset:33792\n\t" \
  "v_mfma_f32_16x16x32_bf16 v[36:39], v[200:203], v[180:183], v[36:39]\n\t" \
  "ds_read_b64_tr_b16 v[156:157], %[b0e1] offset:53248\n\t" \
  "v_mfma_f32_16x16x32_bf16 v[40:43], v[200:203], v[184:187], v[40:43]\n\t" \
  "ds_read_b64_tr_b16 v[158:159], %[b0e1] offset:55296\n\t" \
  "v_mfma_f32_16x16x32_bf16 v[44:47], v[200:203], v[188:191], v[44:47]\n\t" \
  "ds_read_b64_tr_b16 v[160:161], %[b0e0] offset:57344\n\t" \
  "v_mfma_f32_16x16x32_bf16 v[60:63], v[204:207], v[188:191], v[60:63]\n\t" \
  "ds_read_b128 v[136:139], %[a0] offset:34816\n\t" \
  "v_mfma_f32_16x16x32_bf16 v[56:59], v[204:207], v[184:187], v[56:59]\n\t" \
  "ds_read_b64_tr_b16 v[162:163], %[b0e0] offset:59392\n\t" \
  "v_mfma_f32_16x16x32_bf16 v[52:55], v[204:207], v[180:183], v[52:55]\n\t" \
  "ds_read_b64_tr_b16 v[164:165], %[b0e1] offset:57344\n\t" \
  "v_mfma_f32_16x16x32_bf16 v[48:51], v[204:207], v[176:179], v[48:51]\n\t" \
  "ds_read_b64_tr_b16 v[166:167], %[b0e1] offset:59392\n\t" \
  "v_mfma_f32_16x16x32_bf16 v[64:67], v[208:211], v[176:179], v[64:67]\n\t" \
  "ds_read_b128 v[140:143], %[a0] offset:35840\n\t" \
  "v_mfma_f32_16x16x32_bf16 v[68:71], v[208:211], v[180:183], v[68:71]\n\t" \
  "ds_read_b64_tr_b16 v[168:169], %[b0e0] offset:61440\n\t" \
  "v_mfma_f32_16x16x32_bf16 v[72:75], v[208:211], v[184:187], v[72:75]\n\t" \
  "ds_read_b64_tr_b16 v[170:171], %[b0e0] offset:63488\n\t" \
  "v_mfma_f32_16x16x32_bf16 v[76:79], v[208:211], v[188:191], v[76:79]\n\t" \
  "ds_read_b64_tr_b16 v[172:173], %[b0e1] offset:61440\n\t" \
  "v_mfma_f32_16x16x32_bf16 v[92:95], v[212:215], v[188:191], v[92:95]\n\t" \
  "ds_read_b64_tr_b16 v[174:175], %[b0e1] offset:63488\n\t" \
  "v_mfma_f32_16x16x32_bf16 v[88:91], v[212:215], v[184:187], v[88:91]\n\t" \
  "v_mfma_f32_16x16x32_bf16 v[84:87], v[212:215], v[180:183], v[84:87]\n\t" \
  "v_mfma_f32_16x16x32_bf16 v[80:83], v[212:215], v[176:179], v[80:83]\n\t" \
  "v_mfma_f32_16x16x32_bf16 v[96:99], v[216:219], v[176:179], v[96:99]\n\t" \
  "v_mfma_f32_16x16x32_bf16 v[100:103], v[216:219], v[180:183], v[100:103]\n\t" \
  "v_mfma_f32_16x16x32_bf16 v[104:107], v[216:219], v[184:187], v[104:107]\n\t" \
  "v_mfma_f32_16x16x32_bf16 v[108:111], v[216:219], v[188:191], v[108:111]\n\t" \
  "v_mfma_f32_16x16x32_bf16 v[124:127], v[220:223], v[188:191], v[124:127]\n\t" \
  "v_mfma_f32_16x16x32_bf16 v[120:123], v[220:223], v[184:187], v[120:123]\n\t" \
  "v_mfma_f32_16x16x32_bf16 v[116:119], v[220:223], v[180:183], v[116:119]\n\t" \
  "v_mfma_f32_16x16x32_bf16 v[112:115], v[220:223], v[176:179], v[112:115]\n\t" \
  "s_waitcnt lgkmcnt(0)\n\t" \
  "v_mfma_f32_16x16x32_bf16 v[0:3], v[144:147], v[128:131], v[0:3]\n\t" \
  "s_barrier\n\t" \
  "v_mfma_f32_16x16x32_bf16 v[4:7], v[144:147], v[132:135], v[4:7]\n\t" \
  "ds_read_b64_tr_b16 v[192:193], %[b0e0] offset:32768\n\t" \
  "v_mfma_f32_16x16x32_bf16 v[8:11], v[144:147], v[136:139], v[8:11]\n\t" \
  "ds_read_b64_tr_b16 v[194:195], %[b0e0] offset:34816\n\t" \
  "v_mfma_f32_16x16x32_bf16 v[12:15], v[144:147], v[140:143], v[12:15]\n\t" \
  "ds_read_b64_tr_b16 v[196:197], %[b0e1] offset:32768\n\t" \
  "v_mfma_f32_16x16x32_bf16 v[28:31], v[148:151], v[140:143], v[28:31]\n\t" \
  "ds_read_b128 v[176:179], %[a0] offset:40960\n\t" \
  "v_mfma_f32_16x16x32_bf16 v[24:27], v[148:151], v[136:139], v[24:27]\n\t" \
  "ds_read_b64_tr_b16 v[198:199], %[b0e1] offset:34816\n\t" \
  "v_mfma_f32_16x16x32_bf16 v[20:23], v[148:151], v[132:135], v[20:23]\n\t" \
  "ds_read_b64_tr_b16 v[200:201], %[b0e0] offset:36864\n\t" \
  "v_mfma_f32_16x16x32_bf16 v[16:19], v[148:151], v[128:131], v[16:19]\n\t" \
  "ds_read_b64_tr_b16 v[202:203], %[b0e0] offset:38912\n\t" \
  "v_mfma_f32_16x16x32_bf16 v[32:35], v[152:155], v[128:131], v[32:35]\n\t" \
  "ds_read_b128 v[180:183], %[a0] offset:41984\n\t" \
  "v_mfma_f32_16x16x32_bf16 v[36:39], v[152:155], v[132:135], v[36:39]\n\t" \
  "ds_read_b64_tr_b16 v[204:205], %[b0e1] offset:36864\n\t" \
  "v_mfma_f32_16x16x32_bf16 v[40:43], v[152:155], v[136:139], v[40:43]\n\t" \
  "ds_read_b64_tr_b16 v[206:207], %[b0e1] offset:38912\n\t" \
  "v_mfma_f32_16x16x32_bf16 v[44:47], v[152:155], v[140:143], v[44:47]\n\t" \
  "ds_read_b64_tr_b16 v[208:209], %[b0e0] offset:40960\n\t" \
  "v_mfma_f32_16x16x32_bf16 v[60:63], v[156:159], v[140:143], v[60:63]\n\t" \
  "ds_read_b128 v[184:187], %[a0] offset:43008\n\t" \
  "v_mfma_f32_16x16x32_bf16 v[56:59], v[156:159], v[136:139], v[56:59]\n\t" \
  "ds_read_b64_tr_b16 v[210:211], %[b0e0] offset:43008\n\t" \
  "v_mfma_f32_16x16x32_bf16 v[52:55], v[156:159], v[132:135], v[52:55]\n\t" \
  "ds_read_b64_tr_b16 v[212:213], %[b0e1] offset:40960\n\t" \
  "v_mfma_f32_16x16x32_bf16 v[48:51], v[156:159], v[128:131], v[48:51]\n\t" \
  "ds_read_b64_tr_b16 v[214:215], %[b0e1] offset:43008\n\t" \
  "v_mfma_f32_16x16x32_bf16 v[64:67], v[160:163], v[128:131], v[64:67]\n\t" \
  "ds_read_b128 v[188:191], %[a0] offset:44032\n\t" \
  "v_mfma_f32_16x16x32_bf16 v[68:71], v[160:163], v[132:135], v[68:71]\n\t" \
  "ds_read_b64_tr_b16 v[216:217], %[b0e0] offset:45056\n\t" \
  "v_mfma_f32_16x16x32_bf16 v[72:75], v[160:163], v[136:139], v[72:75]\n\t" \
  "ds_read_b64_tr_b16 v[218:219], %[b0e0] offset:47104\n\t" \
  "v_mfma_f32_16x16x32_bf16 v[76:79], v[160:163], v[140:143], v[76:79]\n\t" \
  "ds_read_b64_tr_b16 v[220:221], %[b0e1] offset:45056\n\t" \
  "v_mfma_f32_16x16x32_bf16 v[92:95], v[164:167], v[140:143], v[92:95]\n\t" \
  "ds_read_b64_tr_b16 v[222:223], %[b0e1] offset:47104\n\t" \
  "v_mfma_f32_16x16x32_bf16 v[88:91], v[164:167], v[136:139], v[88:91]\n\t" \
  "v_mfma_f32_16x16x32_bf16 v[84:87], v[164:167], v[132:135], v[84:87]\n\t" \
  "v_mfma_f32_16x16x32_bf16 v[80:83], v[164:167], v[128:131], v[80:83]\n\t" \
  "v_mfma_f32_16x16x32_bf16 v[96:99], v[168:171], v[128:131], v[96:99]\n\t" \
  "v_mfma_f32_16x16x32_bf16 v[100:103], v[168:171], v[132:135], v[100:103]\n\t" \
  "v_mfma_f32_16x16x32_bf16 v[104:107], v[168:171], v[136:139], v[104:107]\n\t" \
  "v_mfma_f32_16x16x32_bf16 v[108:111], v[168:171], v[140:143], v[108:111]\n\t" \
  "v_mfma_f32_16x16x32_bf16 v[124:127], v[172:175], v[140:143], v[124:127]\n\t" \
  "v_mfma_f32_16x16x32_bf16 v[120:123], v[172:175], v[136:139], v[120:123]\n\t" \
  "v_mfma_f32_16x16x32_bf16 v[116:119], v[172:175], v[132:135], v[116:119]\n\t" \
  "v_mfma_f32_16x16x32_bf16 v[112:115], v[172:175], v[128:131], v[112:115]\n\t" \
  "2:\n\t" \
  "s_waitcnt lgkmcnt(0)\n\t" \
  "v_mfma_f32_16x16x32_bf16 v[0:3], v[192:195], v[176:179], v[0:3]\n\t" \
  "s_barrier\n\t" \
  "v_mfma_f32_16x16x32_bf16 v[4:7], v[192:195], v[180:183], v[4:7]\n\t" \
  "ds_read_b64_tr_b16 v[144:145], %[b0e0] offset:16384\n\t" \
  "v_mfma_f32_16x16x32_bf16 v[8:11], v[192:195], v[184:187], v[8:11]\n\t" \
  "ds_read_b64_tr_b16 v[146:147], %[b0e0] offset:18432\n\t" \
  "v_mfma_f32_16x16x32_bf16 v[12:15], v[192:195], v[188:191], v[12:15]\n\t" \
  "ds_read_b64_tr_b16 v[148:149], %[b0e1] offset:16384\n\t" \
  "v_mfma_f32_16x16x32_bf16 v[28:31], v[196:199], v[188:191], v[28:31]\n\t" \
  "ds_read_b128 v[128:131], %[a0] offset:49152\n\t" \
  "v_mfma_f32_16x16x32_bf16 v[24:27], v[196:199], v[184:187], v[24:27]\n\t" \
  "ds_read_b64_tr_b16 v[150:151], %[b0e1] offset:18432\n\t" \
  "v_mfma_f32_16x16x32_bf16 v[20:23], v[196:199], v[180:183], v[20:23]\n\t" \
  "ds_read_b64_tr_b16 v[152:153], %[b0e0] offset:20480\n\t" \
  "v_mfma_f32_16x16x32_bf16 v[16:19], v[196:199], v[176:179], v[16:19]\n\t" \
  "ds_read_b64_tr_b16 v[154:155], %[b0e0] offset:22528\n\t" \
  "v_mfma_f32_16x16x32_bf16 v[32:35], v[200:203], v[176:179], v[32:35]\n\t" \
  "ds_read_b128 v[132:135], %[a0] offset:50176\n\t" \
  "v_mfma_f32_16x16x32_bf16 v[36:39], v[200:203], v[180:183], v[36:39]\n\t" \
  "ds_read_b64_tr_b16 v[156:157], %[b0e1] offset:20480\n\t" \
  "v_mfma_f32_16x16x32_bf16 v[40:43], v[200:203], v[184:187], v[40:43]\n\t" \
  "ds_read_b64_tr_b16 v[158:159], %[b0e1] offset:22528\n\t" \
  "v_mfma_f32_16x16x32_bf16 v[44:47], v[200:203], v[188:191], v[44:47]\n\t" \
  "ds_read_b64_tr_b16 v[160:161], %[b0e0] offset:24576\n\t" \
  "v_mfma_f32_16x16x32_bf16 v[60:63], v[204:207], v[188:191], v[60:63]\n\t" \
  "ds_read_b128 v[136:139], %[a0] offset:51200\n\t" \
  "v_mfma_f32_16x16x32_bf16 v[56:59], v[204:207], v[184:187], v[56:59]\n\t" \
  "ds_read_b64_tr_b16 v[162:163], %[b0e0] offset:26624\n\t" \
  "v_mfma_f32_16x16x32_bf16 v[52:55], v[204:207], v[180:183], v[52:55]\n\t" \
  "ds_read_b64_tr_b16 v[164:165], %[b0e1] offset:24576\n\t" \
  "v_mfma_f32_16x16x32_bf16 v[48:51], v[204:207], v[176:179], v[48:51]\n\t" \
  "ds_read_b64_tr_b16 v[166:167], %[b0e1] offset:26624\n\t" \
  "v_mfma_f32_16x16x32_bf16 v[64:67], v[208:211], v[176:179], v[64:67]\n\t" \
  "ds_read_b128 v[140:143], %[a0] offset:52224\n\t" \
  "v_mfma_f32_16x16x32_bf16 v[68:71], v[208:211], v[180:183], v[68:71]\n\t" \
  "ds_read_b64_tr_b16 v[168:169], %[b0e0] offset:28672\n\t" \
  "v_mfma_f32_16x16x32_bf16 v[72:75], v[208:211], v[184:187], v[72:75]\n\t" \
  "ds_read_b64_tr_b16 v[170:171], %[b0e0] offset:30720\n\t" \
  "v_mfma_f32_16x16x32_bf16 v[76:79], v[208:211], v[188:191], v[76:79]\n\t" \
  "ds_read_b64_tr_b16 v[172:173], %[b0e1] offset:28672\n\t" \
  "v_mfma_f32_16x16x32_bf16 v[92:95], v[212:215], v[188:191], v[92:95]\n\t" \
  "ds_read_b64_tr_b16 v[174:175], %[b0e1] offset:30720\n\t" \
  "v_mfma_f32_16x16x32_bf16 v[88:91], v[212:215], v[184:187], v[88:91]\n\t" \
  "v_mfma_f32_16x16x32_bf16 v[84:87], v[212:215], v[180:183], v[84:87]\n\t" \
  "v_mfma_f32_16x16x32_bf16 v[80:83], v[212:215], v[176:179], v[80:83]\n\t" \
  "v_mfma_f32_16x16x32_bf16 v[96:99], v[216:219], v[176:179], v[96:99]\n\t" \
  "v_mfma_f32_16x16x32_bf16 v[100:103], v[216:219], v[180:183], v[100:103]\n\t" \
  "v_mfma_f32_16x16x32_bf16 v[104:107], v[216:219], v[184:187], v[104:107]\n\t" \
  "v_mfma_f32_16x16x32_bf16 v[108:111], v[216:219], v[188:191], v[108:111]\n\t" \
  "v_mfma_f32_16x16x32_bf16 v[124:127], v[220:223], v[188:191], v[124:127]\n\t" \
  "v_mfma_f32_16x16x32_bf16 v[120:123], v[220:223], v[184:187], v[120:123]\n\t" \
  "v_mfma_f32_16x16x32_bf16 v[116:119], v[220:223], v[180:183], v[116:119]\n\t" \
  "v_mfma_f32_16x16x32_bf16 v[112:115], v[220:223], v[176:179], v[112:115]\n\t" \
  "s_waitcnt lgkmcnt(0)\n\t" \
  "v_mfma_f32_16x16x32_bf16 v[0:3], v[144:147], v[128:131], v[0:3]\n\t" \
  "s_barrier\n\t" \
  "v_mfma_f32_16x16x32_bf16 v[4:7], v[144:147], v[132:135], v[4:7]\n\t" \
  "ds_read_b64_tr_b16 v[192:193], %[b0e0] offset:0\n\t" \
  "v_mfma_f32_16x16x32_bf16 v[8:11], v[144:147], v[136:139], v[8:11]\n\t" \
  "ds_read_b64_tr_b16 v[194:195], %[b0e0] offset:2048\n\t" \
  "v_mfma_f32_16x16x32_bf16 v[12:15], v[144:147], v[140:143], v[12:15]\n\t" \
  "ds_read_b64_tr_b16 v[196:197], %[b0e1] offset:0\n\t" \
  "v_mfma_f32_16x16x32_bf16 v[28:31], v[148:151], v[140:143], v[28:31]\n\t" \
  "ds_read_b128 v[176:179], %[a0] offset:57344\n\t" \
  "v_mfma_f32_16x16x32_bf16 v[24:27], v[148:151], v[136:139], v[24:27]\n\t" \
  "ds_read_b64_tr_b16 v[198:199], %[b0e1] offset:2048\n\t" \
  "v_mfma_f32_16x16x32_bf16 v[20:23], v[148:151], v[132:135], v[20:23]\n\t" \
  "ds_read_b64_tr_b16 v[200:201], %[b0e0] offset:4096\n\t" \
  "v_mfma_f32_16x16x32_bf16 v[16:19], v[148:151], v[128:131], v[16:19]\n\t" \
  "ds_read_b64_tr_b16 v[202:203], %[b0e0] offset:6144\n\t" \
  "v_mfma_f32_16x16x32_bf16 v[32:35], v[152:155], v[128:131], v[32:35]\n\t" \
  "ds_read_b128 v[180:183], %[a0] offset:58368\n\t" \
  "v_mfma_f32_16x16x32_bf16 v[36:39], v[152:155], v[132:135], v[36:39]\n\t" \
  "ds_read_b64_tr_b16 v[204:205], %[b0e1] offset:4096\n\t" \
  "v_mfma_f32_16x16x32_bf16 v[40:43], v[152:155], v[136:139], v[40:43]\n\t" \
  "ds_read_b64_tr_b16 v[206:207], %[b0e1] offset:6144\n\t" \
  "v_mfma_f32_16x16x32_bf16 v[44:47], v[152:155], v[140:143], v[44:47]\n\t" \
  "ds_read_b64_tr_b16 v[208:209], %[b0e0] offset:8192\n\t" \
  "v_mfma_f32_16x16x32_bf16 v[60:63], v[156:159], v[140:143], v[60:63]\n\t" \
  "ds_read_b128 v[184:187], %[a0] offset:59392\n\t" \
  "v_mfma_f32_16x16x32_bf16 v[56:59], v[156:159], v[136:139], v[56:59]\n\t" \
  "ds_read_b64_tr_b16 v[210:211], %[b0e0] offset:10240\n\t" \
  "v_mfma_f32_16x16x32_bf16 v[52:55], v[156:159], v[132:135], v[52:55]\n\t" \
  "ds_read_b64_tr_b16 v[212:213], %[b0e1] offset:8192\n\t" \
  "v_mfma_f32_16x16x32_bf16 v[48:51], v[156:159], v[128:131], v[48:51]\n\t" \
  "ds_read_b64_tr_b16 v[214:215], %[b0e1] offset:10240\n\t" \
  "v_mfma_f32_16x16x32_bf16 v[64:67], v[160:163], v[128:131], v[64:67]\n\t" \
  "ds_read_b128 v[188:191], %[a0] offset:60416\n\t" \
  "v_mfma_f32_16x16x32_bf16 v[68:71], v[160:163], v[132:135], v[68:71]\n\t" \
  "ds_read_b64_tr_b16 v[216:217], %[b0e0] offset:12288\n\t" \
  "v_mfma_f32_16x16x32_bf16 v[72:75], v[160:163], v[136:139], v[72:75]\n\t" \
  "ds_read_b64_tr_b16 v[218:219], %[b0e0] offset:14336\n\t" \
  "v_mfma_f32_16x16x32_bf16 v[76:79], v[160:163], v[140:143], v[76:79]\n\t" \
  "ds_read_b64_tr_b16 v[220:221], %[b0e1] offset:12288\n\t" \
  "v_mfma_f32_16x16x32_bf16 v[92:95], v[164:167], v[140:143], v[92:95]\n\t" \
  "ds_read_b64_tr_b16 v[222:223], %[b0e1] offset:14336\n\t" \
  "v_mfma_f32_16x16x32_bf16 v[88:91], v[164:167], v[136:139], v[88:91]\n\t" \
  "v_mfma_f32_16x16x32_bf16 v[84:87], v[164:167], v[132:135], v[84:87]\n\t" \
  "v_mfma_f32_16x16x32_bf16 v[80:83], v[164:167], v[128:131], v[80:83]\n\t" \
  "v_mfma_f32_16x16x32_bf16 v[96:99], v[168:171], v[128:131], v[96:99]\n\t" \
  "v_mfma_f32_16x16x32_bf16 v[100:103], v[168:171], v[132:135], v[100:103]\n\t" \
  "v_mfma_f32_16x16x32_bf16 v[104:107], v[168:171], v[136:139], v[104:107]\n\t" \
  "v_mfma_f32_16x16x32_bf16 v[108:111], v[168:171], v[140:143], v[108:111]\n\t" \
  "v_mfma_f32_16x16x32_bf16 v[124:127], v[172:175], v[140:143], v[124:127]\n\t" \
  "v_mfma_f32_16x16x32_bf16 v[120:123], v[172:175], v[136:139], v[120:123]\n\t" \
  "v_mfma_f32_16x16x32_bf16 v[116:119], v[172:175], v[132:135], v[116:119]\n\t" \
  "v_mfma_f32_16x16x32_bf16 v[112:115], v[172:175], v[128:131], v[112:115]\n\t" \
  "s_waitcnt lgkmcnt(0)\n\t" \
  "v_mfma_f32_16x16x32_bf16 v[0:3], v[192:195], v[176:179], v[0:3]\n\t" \
  "s_barrier\n\t" \
  "v_mfma_f32_16x16x32_bf16 v[4:7], v[192:195], v[180:183], v[4:7]\n\t" \
  "ds_read_b64_tr_b16 v[144:145], %[b0e0] offset:49152\n\t" \
  "v_mfma_f32_16x16x32_bf16 v[8:11], v[192:195], v[184:187], v[8:11]\n\t" \
  "ds_read_b64_tr_b16 v[146:147], %[b0e0] offset:51200\n\t" \
  "v_mfma_f32_16x16x32_bf16 v[12:15], v[192:195], v[188:191], v[12:15]\n\t" \
  "ds_read_b64_tr_b16 v[148:149], %[b0e1] offset:49152\n\t" \
  "v_mfma_f32_16x16x32_bf16 v[28:31], v[196:199], v[188:191], v[28:31]\n\t" \
  "ds_read_b128 v[128:131], %[a0] offset:0\n\t" \
  "v_mfma_f32_16x16x32_bf16 v[24:27], v[196:199], v[184:187], v[24:27]\n\t" \
  "ds_read_b64_tr_b16 v[150:151], %[b0e1] offset:51200\n\t" \
  "v_mfma_f32_16x16x32_bf16 v[20:23], v[196:199], v[180:183], v[20:23]\n\t" \
  "ds_read_b64_tr_b16 v[152:153], %[b0e0] offset:53248\n\t" \
  "v_mfma_f32_16x16x32_bf16 v[16:19], v[196:199], v[176:179], v[16:19]\n\t" \
  "ds_read_b64_tr_b16 v[154:155], %[b0e0] offset:55296\n\t" \
  "v_mfma_f32_16x16x32_bf16 v[32:35], v[200:203], v[176:179], v[32:35]\n\t" \
  "ds_read_b128 v[132:135], %[a0] offset:1024\n\t" \
  "v_mfma_f32_16x16x32_bf16 v[36:39], v[200:203], v[180:183], v[36:39]\n\t" \
  "ds_read_b64_tr_b16 v[156:157], %[b0e1] offset:53248\n\t" \
  "v_mfma_f32_16x16x32_bf16 v[40:43], v[200:203], v[184:187], v[40:43]\n\t" \
  "ds_read_b64_tr_b16 v[158:159], %[b0e1] offset:55296\n\t" \
  "v_mfma_f32_16x16x32_bf16 v[44:47], v[200:203], v[188:191], v[44:47]\n\t" \
  "ds_read_b64_tr_b16 v[160:161], %[b0e0] offset:57344\n\t" \
  "v_mfma_f32_16x16x32_bf16 v[60:63], v[204:207], v[188:191], v[60:63]\n\t" \
  "ds_read_b128 v[136:139], %[a0] offset:2048\n\t" \
  "v_mfma_f32_16x16x32_bf16 v[56:59], v[204:207], v[184:187], v[56:59]\n\t" \
  "ds_read_b64_tr_b16 v[162:163], %[b0e0] offset:59392\n\t" \
  "v_mfma_f32_16x16x32_bf16 v[52:55], v[204:207], v[180:183], v[52:55]\n\t" \
  "ds_read_b64_tr_b16 v[164:165], %[b0e1] offset:57344\n\t" \
  "v_mfma_f32_16x16x32_bf16 v[48:51], v[204:207], v[176:179], v[48:51]\n\t" \
  "ds_read_b64_tr_b16 v[166:167], %[b0e1] offset:59392\n\t" \
  "v_mfma_f32_16x16x32_bf16 v[64:67], v[208:211], v[176:179], v[64:67]\n\t" \
  "ds_read_b128 v[140:143], %[a0] offset:3072\n\t" \
  "v_mfma_f32_16x16x32_bf16 v[68:71], v[208:211], v[180:183], v[68:71]\n\t" \
  "ds_read_b64_tr_b16 v[168:169], %[b0e0] offset:61440\n\t" \
  "v_mfma_f32_16x16x32_bf16 v[72:75], v[208:211], v[184:187], v[72:75]\n\t" \
  "ds_read_b64_tr_b16 v[170:171], %[b0e0] offset:63488\n\t" \
  "v_mfma_f32_16x16x32_bf16 v[76:79], v[208:211], v[188:191], v[76:79]\n\t" \
  "ds_read_b64_tr_b16 v[172:173], %[b0e1] offset:61440\n\t" \
  "v_mfma_f32_16x16x32_bf16 v[92:95], v[212:215], v[188:191], v[92:95]\n\t" \
  "ds_read_b64_tr_b16 v[174:175], %[b0e1] offset:63488\n\t" \
  "v_mfma_f32_16x16x32_bf16 v[88:91], v[212:215], v[184:187], v[88:91]\n\t" \
  "s_add_i32 %[cnt], %[cnt], -1\n\t" \
  "v_mfma_f32_16x16x32_bf16 v[84:87], v[212:215], v[180:183], v[84:87]\n\t" \
  "s_cmp_lg_u32 %[cnt], 0\n\t" \
  "v_mfma_f32_16x16x32_bf16 v[80:83], v[212:215], v[176:179], v[80:83]\n\t" \
  "v_mfma_f32_16x16x32_bf16 v[96:99], v[216:219], v[176:179], v[96:99]\n\t" \
  "v_mfma_f32_16x16x32_bf16 v[100:103], v[216:219], v[180:183], v[100:103]\n\t" \
  "v_mfma_f32_16x16x32_bf16 v[104:107], v[216:219], v[184:187], v[104:107]\n\t" \
  "v_mfma_f32_16x16x32_bf16 v[108:111], v[216:219], v[188:191], v[108:111]\n\t" \
  "v_mfma_f32_16x16x32_bf16 v[124:127], v[220:223], v[188:191], v[124:127]\n\t" \
  "v_mfma_f32_16x16x32_bf16 v[120:123], v[220:223], v[184:187], v[120:123]\n\t" \
  "v_mfma_f32_16x16x32_bf16 v[116:119], v[220:223], v[180:183], v[116:119]\n\t" \
  "v_mfma_f32_16x16x32_bf16 v[112:115], v[220:223], v[176:179], v[112:115]\n\t" \
  "s_cbranch_scc1 1b\n\t" \
  "s_waitcnt lgkmcnt(0)\n\t" \
  "v_mfma_f32_16x16x32_bf16 v[0:3], v[144:147], v[128:131], v[0:3]\n\t" \
  "ds_read_b128 v[176:179], %[a0] offset:8192\n\t" \
  "v_mfma_f32_16x16x32_bf16 v[4:7], v[144:147], v[132:135], v[4:7]\n\t" \
  "ds_read_b128 v[180:183], %[a0] offset:9216\n\t" \
  "v_mfma_f32_16x16x32_bf16 v[8:11], v[144:147], v[136:139], v[8:11]\n\t" \
  "ds_read_b128 v[184:187], %[a0] offset:10240\n\t" \
  "v_mfma_f32_16x16x32_bf16 v[12:15], v[144:147], v[140:143], v[12:15]\n\t" \
  "ds_read_b128 v[188:191], %[a0] offset:11264\n\t" \
  "v_mfma_f32_16x16x32_bf16 v[28:31], v[148:151], v[140:143], v[28:31]\n\t" \
  "ds_read_b128 v[192:195], %[a0] offset:16384\n\t" \
  "v_mfma_f32_16x16x32_bf16 v[24:27], v[148:151], v[136:139], v[24:27]\n\t" \
  "ds_read_b128 v[196:199], %[a0] offset:17408\n\t" \
  "v_mfma_f32_16x16x32_bf16 v[20:23], v[148:151], v[132:135], v[20:23]\n\t" \
  "ds_read_b128 v[200:203], %[a0] offset:18432\n\t" \
  "v_mfma_f32_16x16x32_bf16 v[16:19], v[148:151], v[128:131], v[16:19]\n\t" \
  "ds_read_b128 v[204:207], %[a0] offset:19456\n\t" \
  "s_waitcnt lgkmcnt(4)\n\t" \
  "v_mfma_f32_16x16x32_bf16 v[0:3], v[152:155], v[176:179], v[0:3]\n\t" \
  "s_barrier\n\t" \
  "ds_read_b64_tr_b16 v[144:145], %[b0e0] offset:32768\n\t" \
  "v_mfma_f32_16x16x32_bf16 v[4:7], v[152:155], v[180:183], v[4:7]\n\t" \
  "ds_read_b64_tr_b16 v[146:147], %[b0e0] offset:34816\n\t" \
  "v_mfma_f32_16x16x32_bf16 v[8:11], v[152:155], v[184:187], v[8:11]\n\t" \
  "ds_read_b64_tr_b16 v[148:149], %[b0e1] offset:32768\n\t" \
  "v_mfma_f32_16x16x32_bf16 v[12:15], v[152:155], v[188:191], v[12:15]\n\t" \
  "ds_read_b64_tr_b16 v[150:151], %[b0e1] offset:34816\n\t" \
  "v_mfma_f32_16x16x32_bf16 v[28:31], v[156:159], v[188:191], v[28:31]\n\t" \
  "ds_read_b128 v[208:211], %[a0] offset:24576\n\t" \
  "v_mfma_f32_16x16x32_bf16 v[24:27], v[156:159], v[184:187], v[24:27]\n\t" \
  "ds_read_b128 v[212:215], %[a0] offset:25600\n\t" \
  "v_mfma_f32_16x16x32_bf16 v[20:23], v[156:159], v[180:183], v[20:23]\n\t" \
  "ds_read_b128 v[216:219], %[a0] offset:26624\n\t" \
  "v_mfma_f32_16x16x32_bf16 v[16:19], v[156:159], v[176:179], v[16:19]\n\t" \
  "ds_read_b128 v[220:223], %[a0] offset:27648\n\t" \
  "s_waitcnt lgkmcnt(8)\n\t" \
  "v_mfma_f32_16x16x32_bf16 v[0:3], v[160:163], v[192:195], v[0:3]\n\t" \
  "ds_read_b64_tr_b16 v[152:153], %[b0e0] offset:36864\n\t" \
  "v_mfma_f32_16x16x32_bf16 v[4:7], v[160:163], v[196:199], v[4:7]\n\t" \
  "ds_read_b64_tr_b16 v[154:155], %[b0e0] offset:38912\n\t" \
  "v_mfma_f32_16x16x32_bf16 v[8:11], v[160:163], v[200:203], v[8:11]\n\t" \
  "ds_read_b64_tr_b16 v[156:157], %[b0e1] offset:36864\n\t" \
  "v_mfma_f32_16x16x32_bf16 v[12:15], v[160:163], v[204:207], v[12:15]\n\t" \
  "ds_read_b64_tr_b16 v[158:159], %[b0e1] offset:38912\n\t" \
  "v_mfma_f32_16x16x32_bf16 v[28:31], v[164:167], v[204:207], v[28:31]\n\t" \
  "ds_read_b128 v[128:131], %[a0] offset:32768\n\t" \
  "v_mfma_f32_16x16x32_bf16 v[24:27], v[164:167], v[200:203], v[24:27]\n\t" \
  "ds_read_b128 v[132:135], %[a0] offset:33792\n\t" \
  "v_mfma_f32_16x16x32_bf16 v[20:23], v[164:167], v[196:199], v[20:23]\n\t" \
  "ds_read_b128 v[136:139], %[a0] offset:34816\n\t" \
  "v_mfma_f32_16x16x32_bf16 v[16:19], v[164:167], v[192:195], v[16:19]\n\t" \
  "ds_read_b128 v[140:143], %[a0] offset:35840\n\t" \
  "s_waitcnt lgkmcnt(8)\n\t" \
  "v_mfma_f32_16x16x32_bf16 v[0:3], v[168:171], v[208:211], v[0:3]\n\t" \
  "ds_read_b64_tr_b16 v[160:161], %[b0e0] offset:40960\n\t" \
  "v_mfma_f32_16x16x32_bf16 v[4:7], v[168:171], v[212:215], v[4:7]\n\t" \
  "ds_read_b64_tr_b16 v[162:163], %[b0e0] offset:43008\n\t" \
  "v_mfma_f32_16x16x32_bf16 v[8:11], v[168:171], v[216:219], v[8:11]\n\t" \
  "ds_read_b64_tr_b16 v[164:165], %[b0e1] offset:40960\n\t" \
  "v_mfma_f32_16x16x32_bf16 v[12:15], v[168:171], v[220:223], v[12:15]\n\t" \
  "ds_read_b64_tr_b16 v[166:167], %[b0e1] offset:43008\n\t" \
  "v_mfma_f32_16x16x32_bf16 v[28:31], v[172:175], v[220:223], v[28:31]\n\t" \
  "ds_read_b128 v[176:179], %[a0] offset:40960\n\t" \
  "v_mfma_f32_16x16x32_bf16 v[24:27], v[172:175], v[216:219], v[24:27]\n\t" \
  "ds_read_b128 v[180:183], %[a0] offset:41984\n\t" \
  "v_mfma_f32_16x16x32_bf16 v[20:23], v[172:175], v[212:215], v[20:23]\n\t" \
  "ds_read_b128 v[184:187], %[a0] offset:43008\n\t" \
  "v_mfma_f32_16x16x32_bf16 v[16:19], v[172:175], v[208:211], v[16:19]\n\t" \
  "ds_read_b128 v[188:191], %[a0] offset:44032\n\t" \
  "s_waitcnt lgkmcnt(8)\n\t" \
  "v_mfma_f32_16x16x32_bf16 v[0:3], v[144:147], v[128:131], v[0:3]\n\t" \
  "ds_read_b64_tr_b16 v[168:169], %[b0e0] offset:45056\n\t" \
  "v_mfma_f32_16x16x32_bf16 v[4:7], v[144:147], v[132:135], v[4:7]\n\t" \
  "ds_read_b64_tr_b16 v[170:171], %[b0e0] offset:47104\n\t" \
  "v_mfma_f32_16x16x32_bf16 v[8:11], v[144:147], v[136:139], v[8:11]\n\t" \
  "ds_read_b64_tr_b16 v[172:173], %[b0e1] offset:45056\n\t" \
  "v_mfma_f32_16x16x32_bf16 v[12:15], v[144:147], v[140:143], v[12:15]\n\t" \
  "ds_read_b64_tr_b16 v[174:175], %[b0e1] offset:47104\n\t" \
  "v_mfma_f32_16x16x32_bf16 v[28:31], v[148:151], v[140:143], v[28:31]\n\t" \
  "ds_read_b128 v[192:195], %[a0] offset:49152\n\t" \
  "v_mfma_f32_16x16x32_bf16 v[24:27], v[148:151], v[136:139], v[24:27]\n\t" \
  "ds_read_b128 v[196:199], %[a0] offset:50176\n\t" \
  "v_mfma_f32_16x16x32_bf16 v[20:23], v[148:151], v[132:135], v[20:23]\n\t" \
  "ds_read_b128 v[200:203], %[a0] offset:51200\n\t" \
  "v_mfma_f32_16x16x32_bf16 v[16:19], v[148:151], v[128:131], v[16:19]\n\t" \
  "ds_read_b128 v[204:207], %[a0] offset:52224\n\t" \
  "s_waitcnt lgkmcnt(4)\n\t" \
  "v_mfma_f32_16x16x32_bf16 v[0:3], v[152:155], v[176:179], v[0:3]\n\t" \
  "s_barrier\n\t" \
  "ds_read_b64_tr_b16 v[144:145], %[b0e0] offset:16384\n\t" \
  "v_mfma_f32_16x16x32_bf16 v[4:7], v[152:155], v[180:183], v[4:7]\n\t" \
  "ds_read_b64_tr_b16 v[146:147], %[b0e0] offset:18432\n\t" \
  "v_mfma_f32_16x16x32_bf16 v[8:11], v[152:155], v[184:187], v[8:11]\n\t" \
  "ds_read_b64_tr_b16 v[148:149], %[b0e1] offset:16384\n\t" \
  "v_mfma_f32_16x16x32_bf16 v[12:15], v[152:155], v[188:191], v[12:15]\n\t" \
  "ds_read_b64_tr_b16 v[150:151], %[b0e1] offset:18432\n\t" \
  "v_mfma_f32_16x16x32_bf16 v[28:31], v[156:159], v[188:191], v[28:31]\n\t" \
  "ds_read_b128 v[208:211], %[a0] offset:57344\n\t" \
  "v_mfma_f32_16x16x32_bf16 v[24:27], v[156:159], v[184:187], v[24:27]\n\t" \
  "ds_read_b128 v[212:215], %[a0] offset:58368\n\t" \
  "v_mfma_f32_16x16x32_bf16 v[20:23], v[156:159], v[180:183], v[20:23]\n\t" \
  "ds_read_b128 v[216:219], %[a0] offset:59392\n\t" \
  "v_mfma_f32_16x16x32_bf16 v[16:19], v[156:159], v[176:179], v[16:19]\n\t" \
  "ds_read_b128 v[220:223], %[a0] offset:60416\n\t" \
  "s_waitcnt lgkmcnt(8)\n\t" \
  "v_mfma_f32_16x16x32_bf16 v[0:3], v[160:163], v[192:195], v[0:3]\n\t" \
  "ds_read_b64_tr_b16 v[152:153], %[b0e0] offset:20480\n\t" \
  "v_mfma_f32_16x16x32_bf16 v[4:7], v[160:163], v[196:199], v[4:7]\n\t" \
  "ds_read_b64_tr_b16 v[154:155], %[b0e0] offset:22528\n\t" \
  "v_mfma_f32_16x16x32_bf16 v[8:11], v[160:163], v[200:203], v[8:11]\n\t" \
  "ds_read_b64_tr_b16 v[156:157], %[b0e1] offset:20480\n\t" \
  "v_mfma_f32_16x16x32_bf16 v[12:15], v[160:163], v[204:207], v[12:15]\n\t" \
  "ds_read_b64_tr_b16 v[158:159], %[b0e1] offset:22528\n\t" \
  "v_mfma_f32_16x16x32_bf16 v[28:31], v[164:167], v[204:207], v[28:31]\n\t" \
  "ds_read_b128 v[128:131], %[a0] offset:0\n\t" \
  "v_mfma_f32_16x16x32_bf16 v[24:27], v[164:167], v[200:203], v[24:27]\n\t" \
  "ds_read_b128 v[132:135], %[a0] offset:1024\n\t" \
  "v_mfma_f32_16x16x32_bf16 v[20:23], v[164:167], v[196:199], v[20:23]\n\t" \
  "ds_read_b128 v[136:139], %[a0] offset:2048\n\t" \
  "v_mfma_f32_16x16x32_bf16 v[16:19], v[164:167], v[192:195], v[16:19]\n\t" \
  "ds_read_b128 v[140:143], %[a0] offset:3072\n\t" \
  "s_waitcnt lgkmcnt(8)\n\t" \
  "v_mfma_f32_16x16x32_bf16 v[0:3], v[168:171], v[208:211], v[0:3]\n\t" \
  "ds_read_b64_tr_b16 v[160:161], %[b0e0] offset:24576\n\t" \
  "v_mfma_f32_16x16x32_bf16 v[4:7], v[168:171], v[212:215], v[4:7]\n\t" \
  "ds_read_b64_tr_b16 v[162:163], %[b0e0] offset:26624\n\t" \
  "v_mfma_f32_16x16x32_bf16 v[8:11], v[168:171], v[216:219], v[8:11]\n\t" \
  "ds_read_b64_tr_b16 v[164:165], %[b0e1] offset:24576\n\t" \
  "v_mfma_f32_16x16x32_bf16 v[12:15], v[168:171], v[220:223], v[12:15]\n\t" \
  "ds_read_b64_tr_b16 v[166:167], %[b0e1] offset:26624\n\t" \
  "v_mfma_f32_16x16x32_bf16 v[28:31], v[172:175], v[220:223], v[28:31]\n\t" \
  "ds_read_b128 v[176:179], %[a0] offset:8192\n\t" \
  "v_mfma_f32_16x16x32_bf16 v[24:27], v[172:175], v[216:219], v[24:27]\n\t" \
  "ds_read_b128 v[180:183], %[a0] offset:9216\n\t" \
  "v_mfma_f32_16x16x32_bf16 v[20:23], v[172:175], v[212:215], v[20:23]\n\t" \
  "ds_read_b128 v[184:187], %[a0] offset:10240\n\t" \
  "v_mfma_f32_16x16x32_bf16 v[16:19], v[172:175], v[208:211], v[16:19]\n\t" \
  "ds_read_b128 v[188:191], %[a0] offset:11264\n\t" \
  "s_waitcnt lgkmcnt(8)\n\t" \
  "v_mfma_f32_16x16x32_bf16 v[32:35], v[144:147], v[128:131], v[32:35]\n\t" \
  "ds_read_b64_tr_b16 v[168:169], %[b0e0] offset:28672\n\t" \
  "v_mfma_f32_16x16x32_bf16 v[36:39], v[144:147], v[132:135], v[36:39]\n\t" \
  "ds_read_b64_tr_b16 v[170:171], %[b0e0] offset:30720\n\t" \
  "v_mfma_f32_16x16x32_bf16 v[40:43], v[144:147], v[136:139], v[40:43]\n\t" \
  "ds_read_b64_tr_b16 v[172:173], %[b0e1] offset:28672\n\t" \
  "v_mfma_f32_16x16x32_bf16 v[44:47], v[144:147], v[140:143], v[44:47]\n\t" \
  "ds_read_b64_tr_b16 v[174:175], %[b0e1] offset:30720\n\t" \
  "v_mfma_f32_16x16x32_bf16 v[60:63], v[148:151], v[140:143], v[60:63]\n\t" \
  "ds_read_b128 v[192:195], %[a0] offset:16384\n\t" \
  "v_cvt_pk_bf16_f32 v0, v0, v1\n\t" \
  "v_cvt_pk_bf16_f32 v1, v2, v3\n\t" \
  "v_mfma_f32_16x16x32_bf16 v[56:59], v[148:151], v[136:139], v[56:59]\n\t" \
  "ds_read_b128 v[196:199], %[a0] offset:17408\n\t" \
  "v_cvt_pk_bf16_f32 v2, v16, v17\n\t" \
  "v_cvt_pk_bf16_f32 v3, v18, v19\n\t" \
  "v_mfma_f32_16x16x32_bf16 v[52:55], v[148:151], v[132:135], v[52:55]\n\t" \
  "ds_read_b128 v[200:203], %[a0] offset:18432\n\t" \
  "buffer_store_dwordx4 v[0:3], %[vx], %[rs], %[x0] offen offset:0 sc1\n\t" \
  "v_cvt_pk_bf16_f32 v4, v4, v5\n\t" \
  "v_mfma_f32_16x16x32_bf16 v[48:51], v[148:151], v[128:131], v[48:51]\n\t" \
  "ds_read_b128 v[204:207], %[a0] offset:19456\n\t" \
  "v_cvt_pk_bf16_f32 v5, v6, v7\n\t" \
  "v_cvt_pk_bf16_f32 v6, v20, v21\n\t" \
  "s_waitcnt lgkmcnt(4)\n\t" \
  "v_mfma_f32_16x16x32_bf16 v[32:35], v[152:155], v[176:179], v[32:35]\n\t" \
  "s_barrier\n\t" \
  "ds_read_b64_tr_b16 v[144:145], %[b0e0] offset:0\n\t" \
  "v_cvt_pk_bf16_f32 v7, v22, v23\n\t" \
  "buffer_store_dwordx4 v[4:7], %[vx], %[rs], %[x0] offen offset:1024 sc1\n\t" \
  "v_mfma_f32_16x16x32_bf16 v[36:39], v[152:155], v[180:183], v[36:39]\n\t" \
  "ds_read_b64_tr_b16 v[146:147], %[b0e0] offset:2048\n\t" \
  "v_cvt_pk_bf16_f32 v8, v8, v9\n\t" \
  "v_cvt_pk_bf16_f32 v9, v10, v11\n\t" \
  "v_mfma_f32_16x16x32_bf16 v[40:43], v[152:155], v[184:187], v[40:43]\n\t" \
  "ds_read_b64_tr_b16 v[148:149], %[b0e1] offset:0\n\t" \
  "v_cvt_pk_bf16_f32 v10, v24, v25\n\t" \
  "v_cvt_pk_bf16_f32 v11, v26, v27\n\t" \
  "v_mfma_f32_16x16x32_bf16 v[44:47], v[152:155], v[188:191], v[44:47]\n\t" \
  "ds_read_b64_tr_b16 v[150:151], %[b0e1] offset:2048\n\t" \
  "buffer_store_dwordx4 v[8:11], %[vx], %[rs], %[x0] offen offset:2048 sc1\n\t" \
  "v_cvt_pk_bf16_f32 v12, v12, v13\n\t" \
  "v_mfma_f32_16x16x32_bf16 v[60:63], v[156:159], v[188:191], v[60:63]\n\t" \
  "ds_read_b128 v[208:211], %[a0] offset:24576\n\t" \
  "v_cvt_pk_bf16_f32 v13, v14, v15\n\t" \
  "v_cvt_pk_bf16_f32 v14, v28, v29\n\t" \
  "v_mfma_f32_16x16x32_bf16 v[56:59], v[156:159], v[184:187], v[56:59]\n\t" \
  "ds_read_b128 v[212:215], %[a0] offset:25600\n\t" \
  "v_cvt_pk_bf16_f32 v15, v30, v31\n\t" \
  "buffer_store_dwordx4 v[12:15], %[vx], %[rs], %[x0] offen offset:3072 sc1\n\t" \
  "v_mfma_f32_16x16x32_bf16 v[52:55], v[156:159], v[180:183], v[52:55]\n\t" \
  "ds_read_b128 v[216:219], %[a0] offset:26624\n\t" \
  "v_mfma_f32_16x16x32_bf16 v[48:51], v[156:159], v[176:179], v[48:51]\n\t" \
  "ds_read_b128 v[220:223], %[a0] offset:27648\n\t" \
  "s_waitcnt lgkmcnt(8)\n\t" \
  "v_mfma_f32_16x16x32_bf16 v[32:35], v[160:163], v[192:195], v[32:35]\n\t" \
  "ds_read_b64_tr_b16 v[152:153], %[b0e0] offset:4096\n\t" \
  "v_mfma_f32_16x16x32_bf16 v[36:39], v[160:163], v[196:199], v[36:39]\n\t" \
  "ds_read_b64_tr_b16 v[154:155], %[b0e0] offset:6144\n\t" \
  "v_mfma_f32_16x16x32_bf16 v[40:43], v[160:163], v[200:203], v[40:43]\n\t" \
  "ds_read_b64_tr_b16 v[156:157], %[b0e1] offset:4096\n\t" \
  "v_mfma_f32_16x16x32_bf16 v[44:47], v[160:163], v[204:207], v[44:47]\n\t" \
  "ds_read_b64_tr_b16 v[158:159], %[b0e1] offset:6144\n\t" \
  "v_mfma_f32_16x16x32_bf16 v[60:63], v[164:167], v[204:207], v[60:63]\n\t" \
  "ds_read_b128 v[128:131], %[a0] offset:32768\n\t" \
  "v_mfma_f32_16x16x32_bf16 v[56:59], v[164:167], v[200:203], v[56:59]\n\t" \
  "ds_read_b128 v[132:135], %[a0] offset:33792\n\t" \
  "v_mfma_f32_16x16x32_bf16 v[52:55], v[164:167], v[196:199], v[52:55]\n\t" \
  "ds_read_b128 v[136:139], %[a0] offset:34816\n\t" \
  "v_mfma_f32_16x16x32_bf16 v[48:51], v[164:167], v[192:195], v[48:51]\n\t" \
  "ds_read_b128 v[140:143], %[a0] offset:35840\n\t" \
  "s_waitcnt lgkmcnt(8)\n\t" \
  "v_mfma_f32_16x16x32_bf16 v[32:35], v[168:171], v[208:211], v[32:35]\n\t" \
  "ds_read_b64_tr_b16 v[160:161], %[b0e0] offset:8192\n\t" \
  "v_mfma_f32_16x16x32_bf16 v[36:39], v[168:171], v[212:215], v[36:39]\n\t" \
  "ds_read_b64_tr_b16 v[162:163], %[b0e0] offset:10240\n\t" \
  "v_mfma_f32_16x16x32_bf16 v[40:43], v[168:171], v[216:219], v[40:43]\n\t" \
  "ds_read_b64_tr_b16 v[164:165], %[b0e1] offset:8192\n\t" \
  "v_mfma_f32_16x16x32_bf16 v[44:47], v[168:171], v[220:223], v[44:47]\n\t" \
  "ds_read_b64_tr_b16 v[166:167], %[b0e1] offset:10240\n\t" \
  "v_mfma_f32_16x16x32_bf16 v[60:63], v[172:175], v[220:223], v[60:63]\n\t" \
  "ds_read_b128 v[176:179], %[a0] offset:40960\n\t" \
  "v_mfma_f32_16x16x32_bf16 v[56:59], v[172:175], v[216:219], v[56:59]\n\t" \
  "ds_read_b128 v[180:183], %[a0] offset:41984\n\t" \
  "v_mfma_f32_16x16x32_bf16 v[52:55], v[172:175], v[212:215], v[52:55]\n\t" \
  "ds_read_b128 v[184:187], %[a0] offset:43008\n\t" \
  "v_mfma_f32_16x16x32_bf16 v[48:51], v[172:175], v[208:211], v[48:51]\n\t" \
  "ds_read_b128 v[188:191], %[a0] offset:44032\n\t" \
  "s_waitcnt lgkmcnt(8)\n\t" \
  "v_mfma_f32_16x16x32_bf16 v[32:35], v[144:147], v[128:131], v[32:35]\n\t" \
  "ds_read_b64_tr_b16 v[168:169], %[b0e0] offset:12288\n\t" \
  "v_mfma_f32_16x16x32_bf16 v[36:39], v[144:147], v[132:135], v[36:39]\n\t" \
  "ds_read_b64_tr_b16 v[170:171], %[b0e0] offset:14336\n\t" \
  "v_mfma_f32_16x16x32_bf16 v[40:43], v[144:147], v[136:139], v[40:43]\n\t" \
  "ds_read_b64_tr_b16 v[172:173], %[b0e1] offset:12288\n\t" \
  "v_mfma_f32_16x16x32_bf16 v[44:47], v[144:147], v[140:143], v[44:47]\n\t" \
  "ds_read_b64_tr_b16 v[174:175], %[b0e1] offset:14336\n\t" \
  "v_mfma_f32_16x16x32_bf16 v[60:63], v[148:151], v[140:143], v[60:63]\n\t" \
  "ds_read_b128 v[192:195], %[a0] offset:49152\n\t" \
  "v_mfma_f32_16x16x32_bf16 v[56:59], v[148:151], v[136:139], v[56:59]\n\t" \
  "ds_read_b128 v[196:199], %[a0] offset:50176\n\t" \
  "v_mfma_f32_16x16x32_bf16 v[52:55], v[148:151], v[132:135], v[52:55]\n\t" \
  "ds_read_b128 v[200:203], %[a0] offset:51200\n\t" \
  "v_mfma_f32_16x16x32_bf16 v[48:51], v[148:151], v[128:131], v[48:51]\n\t" \
  "ds_read_b128 v[204:207], %[a0] offset:52224\n\t" \
  "s_waitcnt lgkmcnt(4)\n\t" \
  "v_mfma_f32_16x16x32_bf16 v[32:35], v[152:155], v[176:179], v[32:35]\n\t" \
  "s_barrier\n\t" \
  "ds_read_b64_tr_b16 v[144:145], %[b0e0] offset:49152\n\t" \
  "v_mfma_f32_16x16x32_bf16 v[36:39], v[152:155], v[180:183], v[36:39]\n\t" \
  "ds_read_b64_tr_b16 v[146:147], %[b0e0] offset:51200\n\t" \
  "v_mfma_f32_16x16x32_bf16 v[40:43], v[152:155], v[184:187], v[40:43]\n\t" \
  "ds_read_b64_tr_b16 v[148:149], %[b0e1] offset:49152\n\t" \
  "v_mfma_f32_16x16x32_bf16 v[44:47], v[152:155], v[188:191], v[44:47]\n\t" \
  "ds_read_b64_tr_b16 v[150:151], %[b0e1] offset:51200\n\t" \
  "v_mfma_f32_16x16x32_bf16 v[60:63], v[156:159], v[188:191], v[60:63]\n\t" \
  "ds_read_b128 v[208:211], %[a0] offset:57344\n\t" \
  "v_mfma_f32_16x16x32_bf16 v[56:59], v[156:159], v[184:187], v[56:59]\n\t" \
  "ds_read_b128 v[212:215], %[a0] offset:58368\n\t" \
  "v_mfma_f32_16x16x32_bf16 v[52:55], v[156:159], v[180:183], v[52:55]\n\t" \
  "ds_read_b128 v[216:219], %[a0] offset:59392\n\t" \
  "v_mfma_f32_16x16x32_bf16 v[48:51], v[156:159], v[176:179], v[48:51]\n\t" \
  "ds_read_b128 v[220:223], %[a0] offset:60416\n\t" \
  "s_waitcnt lgkmcnt(8)\n\t" \
  "v_mfma_f32_16x16x32_bf16 v[32:35], v[160:163], v[192:195], v[32:35]\n\t" \
  "ds_read_b64_tr_b16 v[152:153], %[b0e0] offset:53248\n\t" \
  "v_mfma_f32_16x16x32_bf16 v[36:39], v[160:163], v[196:199], v[36:39]\n\t" \
  "ds_read_b64_tr_b16 v[154:155], %[b0e0] offset:55296\n\t" \
  "v_mfma_f32_16x16x32_bf16 v[40:43], v[160:163], v[200:203], v[40:43]\n\t" \
  "ds_read_b64_tr_b16 v[156:157], %[b0e1] offset:53248\n\t" \
  "v_mfma_f32_16x16x32_bf16 v[44:47], v[160:163], v[204:207], v[44:47]\n\t" \
  "ds_read_b64_tr_b16 v[158:159], %[b0e1] offset:55296\n\t" \
  "v_mfma_f32_16x16x32_bf16 v[60:63], v[164:167], v[204:207], v[60:63]\n\t" \
  "ds_read_b128 v[128:131], %[a0] offset:0\n\t" \
  "v_mfma_f32_16x16x32_bf16 v[56:59], v[164:167], v[200:203], v[56:59]\n\t" \
  "ds_read_b128 v[132:135], %[a0] offset:1024\n\t" \
  "v_mfma_f32_16x16x32_bf16 v[52:55], v[164:167], v[196:199], v[52:55]\n\t" \
  "ds_read_b128 v[136:139], %[a0] offset:2048\n\t" \
  "v_mfma_f32_16x16x32_bf16 v[48:51], v[164:167], v[192:195], v[48:51]\n\t" \
  "ds_read_b128 v[140:143], %[a0] offset:3072\n\t" \
  "s_waitcnt lgkmcnt(8)\n\t" \
  "v_mfma_f32_16x16x32_bf16 v[32:35], v[168:171], v[208:211], v[32:35]\n\t" \
  "ds_read_b64_tr_b16 v[160:161], %[b0e0] offset:57344\n\t" \
  "v_mfma_f32_16x16x32_bf16 v[36:39], v[168:171], v[212:215], v[36:39]\n\t" \
  "ds_read_b64_tr_b16 v[162:163], %[b0e0] offset:59392\n\t" \
  "v_mfma_f32_16x16x32_bf16 v[40:43], v[168:171], v[216:219], v[40:43]\n\t" \
  "ds_read_b64_tr_b16 v[164:165], %[b0e1] offset:57344\n\t" \
  "v_mfma_f32_16x16x32_bf16 v[44:47], v[168:171], v[220:223], v[44:47]\n\t" \
  "ds_read_b64_tr_b16 v[166:167], %[b0e1] offset:59392\n\t" \
  "v_mfma_f32_16x16x32_bf16 v[60:63], v[172:175], v[220:223], v[60:63]\n\t" \
  "ds_read_b128 v[176:179], %[a0] offset:8192\n\t" \
  "v_mfma_f32_16x16x32_bf16 v[56:59], v[172:175], v[216:219], v[56:59]\n\t" \
  "ds_read_b128 v[180:183], %[a0] offset:9216\n\t" \
  "v_mfma_f32_16x16x32_bf16 v[52:55], v[172:175], v[212:215], v[52:55]\n\t" \
  "ds_read_b128 v[184:187], %[a0] offset:10240\n\t" \
  "v_mfma_f32_16x16x32_bf16 v[48:51], v[172:175], v[208:211], v[48:51]\n\t" \
  "ds_read_b128 v[188:191], %[a0] offset:11264\n\t" \
  "s_waitcnt lgkmcnt(8)\n\t" \
  "v_mfma_f32_16x16x32_bf16 v[64:67], v[144:147], v[128:131], v[64:67]\n\t" \
  "ds_read_b64_tr_b16 v[168:169], %[b0e0] offset:61440\n\t" \
  "v_mfma_f32_16x16x32_bf16 v[68:71], v[144:147], v[132:135], v[68:71]\n\t" \
  "ds_read_b64_tr_b16 v[170:171], %[b0e0] offset:63488\n\t" \
  "v_mfma_f32_16x16x32_bf16 v[72:75], v[144:147], v[136:139], v[72:75]\n\t" \
  "ds_read_b64_tr_b16 v[172:173], %[b0e1] offset:61440\n\t" \
  "v_mfma_f32_16x16x32_bf16 v[76:79], v[144:147], v[140:143], v[76:79]\n\t" \
  "ds_read_b64_tr_b16 v[174:175], %[b0e1] offset:63488\n\t" \
  "v_mfma_f32_16x16x32_bf16 v[92:95], v[148:151], v[140:143], v[92:95]\n\t" \
  "ds_read_b128 v[192:195], %[a0] offset:16384\n\t" \
  "v_cvt_pk_bf16_f32 v32, v32, v33\n\t" \
  "v_cvt_pk_bf16_f32 v33, v34, v35\n\t" \
  "v_mfma_f32_16x16x32_bf16 v[88:91], v[148:151], v[136:139], v[88:91]\n\t" \
  "ds_read_b128 v[196:199], %[a0] offset:17408\n\t" \
  "v_cvt_pk_bf16_f32 v34, v48, v49\n\t" \
  "v_cvt_pk_bf16_f32 v35, v50, v51\n\t" \
  "v_mfma_f32_16x16x32_bf16 v[84:87], v[148:151], v[132:135], v[84:87]\n\t" \
  "ds_read_b128 v[200:203], %[a0] offset:18432\n\t" \
  "buffer_store_dwordx4 v[32:35], %[vx], %[rs], %[x1] offen offset:0 sc1\n\t" \
  "v_cvt_pk_bf16_f32 v36, v36, v37\n\t" \
  "v_mfma_f32_16x16x32_bf16 v[80:83], v[148:151], v[128:131], v[80:83]\n\t" \
  "ds_read_b128 v[204:207], %[a0] offset:19456\n\t" \
  "v_cvt_pk_bf16_f32 v37, v38, v39\n\t" \
  "v_cvt_pk_bf16_f32 v38, v52, v53\n\t" \
  "s_waitcnt lgkmcnt(4)\n\t" \
  "v_mfma_f32_16x16x32_bf16 v[64:67], v[152:155], v[176:179], v[64:67]\n\t" \
  "s_barrier\n\t" \
  "ds_read_b64_tr_b16 v[144:145], %[b0e0] offset:32768\n\t" \
  "v_cvt_pk_bf16_f32 v39, v54, v55\n\t" \
  "buffer_store_dwordx4 v[36:39], %[vx], %[rs], %[x1] offen offset:1024 sc1\n\t" \
  "v_mfma_f32_16x16x32_bf16 v[68:71], v[152:155], v[180:183], v[68:71]\n\t" \
  "ds_read_b64_tr_b16 v[146:147], %[b0e0] offset:34816\n\t" \
  "v_cvt_pk_bf16_f32 v40, v40, v41\n\t" \
  "v_cvt_pk_bf16_f32 v41, v42, v43\n\t" \
  "v_mfma_f32_16x16x32_bf16 v[72:75], v[152:155], v[184:187], v[72:75]\n\t" \
  "ds_read_b64_tr_b16 v[148:149], %[b0e1] offset:32768\n\t" \
  "v_cvt_pk_bf16_f32 v42, v56, v57\n\t" \
  "v_cvt_pk_bf16_f32 v43, v58, v59\n\t" \
  "v_mfma_f32_16x16x32_bf16 v[76:79], v[152:155], v[188:191], v[76:79]\n\t" \
  "ds_read_b64_tr_b16 v[150:151], %[b0e1] offset:34816\n\t" \
  "buffer_store_dwordx4 v[40:43], %[vx], %[rs], %[x1] offen offset:2048 sc1\n\t" \
  "v_cvt_pk_bf16_f32 v44, v44, v45\n\t" \
  "v_mfma_f32_16x16x32_bf16 v[92:95], v[156:159], v[188:191], v[92:95]\n\t" \
  "ds_read_b128 v[208:211], %[a0] offset:24576\n\t" \
  "v_cvt_pk_bf16_f32 v45, v46, v47\n\t" \
  "v_cvt_pk_bf16_f32 v46, v60, v61\n\t" \
  "v_mfma_f32_16x16x32_bf16 v[88:91], v[156:159], v[184:187], v[88:91]\n\t" \
  "ds_read_b128 v[212:215], %[a0] offset:25600\n\t" \
  "v_cvt_pk_bf16_f32 v47, v62, v63\n\t" \
  "buffer_store_dwordx4 v[44:47], %[vx], %[rs], %[x1] offen offset:3072 sc1\n\t" \
  "v_mfma_f32_16x16x32_bf16 v[84:87], v[156:159], v[180:183], v[84:87]\n\t" \
  "ds_read_b128 v[216:219], %[a0] offset:26624\n\t" \
  "s_waitcnt vmcnt(4)\n\t" \
  "buffer_store_dwordx2 %[tg], %[vf], %[rs], %[f0] offen sc1\n\t" \
  "v_mfma_f32_16x16x32_bf16 v[80:83], v[156:159], v[176:179], v[80:83]\n\t" \
  "ds_read_b128 v[220:223], %[a0] offset:27648\n\t" \
  "s_waitcnt lgkmcnt(8)\n\t" \
  "v_mfma_f32_16x16x32_bf16 v[64:67], v[160:163], v[192:195], v[64:67]\n\t" \
  "ds_read_b64_tr_b16 v[152:153], %[b0e0] offset:36864\n\t" \
  "v_mfma_f32_16x16x32_bf16 v[68:71], v[160:163], v[196:199], v[68:71]\n\t" \
  "ds_read_b64_tr_b16 v[154:155], %[b0e0] offset:38912\n\t" \
  "v_mfma_f32_16x16x32_bf16 v[72:75], v[160:163], v[200:203], v[72:75]\n\t" \
  "ds_read_b64_tr_b16 v[156:157], %[b0e1] offset:36864\n\t" \
  "v_mfma_f32_16x16x32_bf16 v[76:79], v[160:163], v[204:207], v[76:79]\n\t" \
  "ds_read_b64_tr_b16 v[158:159], %[b0e1] offset:38912\n\t" \
  "v_mfma_f32_16x16x32_bf16 v[92:95], v[164:167], v[204:207], v[92:95]\n\t" \
  "ds_read_b128 v[128:131], %[a0] offset:32768\n\t" \
  "v_mfma_f32_16x16x32_bf16 v[88:91], v[164:167], v[200:203], v[88:91]\n\t" \
  "ds_read_b128 v[132:135], %[a0] offset:33792\n\t" \
  "v_mfma_f32_16x16x32_bf16 v[84:87], v[164:167], v[196:199], v[84:87]\n\t" \
  "ds_read_b128 v[136:139], %[a0] offset:34816\n\t" \
  "v_mfma_f32_16x16x32_bf16 v[80:83], v[164:167], v[192:195], v[80:83]\n\t" \
  "ds_read_b128 v[140:143], %[a0] offset:35840\n\t" \
  "s_waitcnt lgkmcnt(8)\n\t" \
  "v_mfma_f32_16x16x32_bf16 v[64:67], v[168:171], v[208:211], v[64:67]\n\t" \
  "ds_read_b64_tr_b16 v[160:161], %[b0e0] offset:40960\n\t" \
  "v_mfma_f32_16x16x32_bf16 v[68:71], v[168:171], v[212:215], v[68:71]\n\t" \
  "ds_read_b64_tr_b16 v[162:163], %[b0e0] offset:43008\n\t" \
  "v_mfma_f32_16x16x32_bf16 v[72:75], v[168:171], v[216:219], v[72:75]\n\t" \
  "ds_read_b64_tr_b16 v[164:165], %[b0e1] offset:40960\n\t" \
  "v_mfma_f32_16x16x32_bf16 v[76:79], v[168:171], v[220:223], v[76:79]\n\t" \
  "ds_read_b64_tr_b16 v[166:167], %[b0e1] offset:43008\n\t" \
  "v_mfma_f32_16x16x32_bf16 v[92:95], v[172:175], v[220:223], v[92:95]\n\t" \
  "ds_read_b128 v[176:179], %[a0] offset:40960\n\t" \
  "v_mfma_f32_16x16x32_bf16 v[88:91], v[172:175], v[216:219], v[88:91]\n\t" \
  "ds_read_b128 v[180:183], %[a0] offset:41984\n\t" \
  "v_mfma_f32_16x16x32_bf16 v[84:87], v[172:175], v[212:215], v[84:87]\n\t" \
  "ds_read_b128 v[184:187], %[a0] offset:43008\n\t" \
  "v_mfma_f32_16x16x32_bf16 v[80:83], v[172:175], v[208:211], v[80:83]\n\t" \
  "ds_read_b128 v[188:191], %[a0] offset:44032\n\t" \
  "s_waitcnt lgkmcnt(8)\n\t" \
  "v_mfma_f32_16x16x32_bf16 v[64:67], v[144:147], v[128:131], v[64:67]\n\t" \
  "ds_read_b64_tr_b16 v[168:169], %[b0e0] offset:45056\n\t" \
  "v_mfma_f32_16x16x32_bf16 v[68:71], v[144:147], v[132:135], v[68:71]\n\t" \
  "ds_read_b64_tr_b16 v[170:171], %[b0e0] offset:47104\n\t" \
  "v_mfma_f32_16x16x32_bf16 v[72:75], v[144:147], v[136:139], v[72:75]\n\t" \
  "ds_read_b64_tr_b16 v[172:173], %[b0e1] offset:45056\n\t" \
  "v_mfma_f32_16x16x32_bf16 v[76:79], v[144:147], v[140:143], v[76:79]\n\t" \
  "ds_read_b64_tr_b16 v[174:175], %[b0e1] offset:47104\n\t" \
  "v_mfma_f32_16x16x32_bf16 v[92:95], v[148:151], v[140:143], v[92:95]\n\t" \
  "ds_read_b128 v[192:195], %[a0] offset:49152\n\t" \
  "v_mfma_f32_16x16x32_bf16 v[88:91], v[148:151], v[136:139], v[88:91]\n\t" \
  "ds_read_b128 v[196:199], %[a0] offset:50176\n\t" \
  "v_mfma_f32_16x16x32_bf16 v[84:87], v[148:151], v[132:135], v[84:87]\n\t" \
  "ds_read_b128 v[200:203], %[a0] offset:51200\n\t" \
  "v_mfma_f32_16x16x32_bf16 v[80:83], v[148:151], v[128:131], v[80:83]\n\t" \
  "ds_read_b128 v[204:207], %[a0] offset:52224\n\t" \
  "s_waitcnt lgkmcnt(4)\n\t" \
  "v_mfma_f32_16x16x32_bf16 v[64:67], v[152:155], v[176:179], v[64:67]\n\t" \
  "s_barrier\n\t" \
  "ds_read_b64_tr_b16 v[144:145], %[b0e0] offset:16384\n\t" \
  "v_mfma_f32_16x16x32_bf16 v[68:71], v[152:155], v[180:183], v[68:71]\n\t" \
  "ds_read_b64_tr_b16 v[146:147], %[b0e0] offset:18432\n\t" \
  "v_mfma_f32_16x16x32_bf16 v[72:75], v[152:155], v[184:187], v[72:75]\n\t" \
  "ds_read_b64_tr_b16 v[148:149], %[b0e1] offset:16384\n\t" \
  "v_mfma_f32_16x16x32_bf16 v[76:79], v[152:155], v[188:191], v[76:79]\n\t" \
  "ds_read_b64_tr_b16 v[150:151], %[b0e1] offset:18432\n\t" \
  "v_mfma_f32_16x16x32_bf16 v[92:95], v[156:159], v[188:191], v[92:95]\n\t" \
  "ds_read_b128 v[208:211], %[a0] offset:57344\n\t" \
  "v_mfma_f32_16x16x32_bf16 v[88:91], v[156:159], v[184:187], v[88:91]\n\t" \
  "ds_read_b128 v[212:215], %[a0] offset:58368\n\t" \
  "v_mfma_f32_16x16x32_bf16 v[84:87], v[156:159], v[180:183], v[84:87]\n\t" \
  "ds_read_b128 v[216:219], %[a0] offset:59392\n\t" \
  "v_mfma_f32_16x16x32_bf16 v[80:83], v[156:159], v[176:179], v[80:83]\n\t" \
  "ds_read_b128 v[220:223], %[a0] offset:60416\n\t" \
  "s_waitcnt lgkmcnt(8)\n\t" \
  "v_mfma_f32_16x16x32_bf16 v[64:67], v[160:163], v[192:195], v[64:67]\n\t" \
  "ds_read_b64_tr_b16 v[152:153], %[b0e0] offset:20480\n\t" \
  "v_mfma_f32_16x16x32_bf16 v[68:71], v[160:163], v[196:199], v[68:71]\n\t" \
  "ds_read_b64_tr_b16 v[154:155], %[b0e0] offset:22528\n\t" \
  "v_mfma_f32_16x16x32_bf16 v[72:75], v[160:163], v[200:203], v[72:75]\n\t" \
  "ds_read_b64_tr_b16 v[156:157], %[b0e1] offset:20480\n\t" \
  "v_mfma_f32_16x16x32_bf16 v[76:79], v[160:163], v[204:207], v[76:79]\n\t" \
  "ds_read_b64_tr_b16 v[158:159], %[b0e1] offset:22528\n\t" \
  "v_mfma_f32_16x16x32_bf16 v[92:95], v[164:167], v[204:207], v[92:95]\n\t" \
  "ds_read_b128 v[128:131], %[a0] offset:0\n\t" \
  "v_mfma_f32_16x16x32_bf16 v[88:91], v[164:167], v[200:203], v[88:91]\n\t" \
  "ds_read_b128 v[132:135], %[a0] offset:1024\n\t" \
  "v_mfma_f32_16x16x32_bf16 v[84:87], v[164:167], v[196:199], v[84:87]\n\t" \
  "ds_read_b128 v[136:139], %[a0] offset:2048\n\t" \
  "v_mfma_f32_16x16x32_bf16 v[80:83], v[164:167], v[192:195], v[80:83]\n\t" \
  "ds_read_b128 v[140:143], %[a0] offset:3072\n\t" \
  "s_waitcnt lgkmcnt(8)\n\t" \
  "v_mfma_f32_16x16x32_bf16 v[64:67], v[168:171], v[208:211], v[64:67]\n\t" \
  "ds_read_b64_tr_b16 v[160:161], %[b0e0] offset:24576\n\t" \
  "v_mfma_f32_16x16x32_bf16 v[68:71], v[168:171], v[212:215], v[68:71]\n\t" \
  "ds_read_b64_tr_b16 v[162:163], %[b0e0] offset:26624\n\t" \
  "v_mfma_f32_16x16x32_bf16 v[72:75], v[168:171], v[216:219], v[72:75]\n\t" \
  "ds_read_b64_tr_b16 v[164:165], %[b0e1] offset:24576\n\t" \
  "v_mfma_f32_16x16x32_bf16 v[76:79], v[168:171], v[220:223], v[76:79]\n\t" \
  "ds_read_b64_tr_b16 v[166:167], %[b0e1] offset:26624\n\t" \
  "v_mfma_f32_16x16x32_bf16 v[92:95], v[172:175], v[220:223], v[92:95]\n\t" \
  "ds_read_b128 v[176:179], %[a0] offset:8192\n\t" \
  "v_mfma_f32_16x16x32_bf16 v[88:91], v[172:175], v[216:219], v[88:91]\n\t" \
  "ds_read_b128 v[180:183], %[a0] offset:9216\n\t" \
  "v_mfma_f32_16x16x32_bf16 v[84:87], v[172:175], v[212:215], v[84:87]\n\t" \
  "ds_read_b128 v[184:187], %[a0] offset:10240\n\t" \
  "v_mfma_f32_16x16x32_bf16 v[80:83], v[172:175], v[208:211], v[80:83]\n\t" \
  "ds_read_b128 v[188:191], %[a0] offset:11264\n\t" \
  "s_waitcnt lgkmcnt(8)\n\t" \
  "v_mfma_f32_16x16x32_bf16 v[96:99], v[144:147], v[128:131], v[96:99]\n\t" \
  "ds_read_b64_tr_b16 v[168:169], %[b0e0] offset:28672\n\t" \
  "v_mfma_f32_16x16x32_bf16 v[100:103], v[144:147], v[132:135], v[100:103]\n\t" \
  "ds_read_b64_tr_b16 v[170:171], %[b0e0] offset:30720\n\t" \
  "v_mfma_f32_16x16x32_bf16 v[104:107], v[144:147], v[136:139], v[104:107]\n\t" \
  "ds_read_b64_tr_b16 v[172:173], %[b0e1] offset:28672\n\t" \
  "v_mfma_f32_16x16x32_bf16 v[108:111], v[144:147], v[140:143], v[108:111]\n\t" \
  "ds_read_b64_tr_b16 v[174:175], %[b0e1] offset:30720\n\t" \
  "v_mfma_f32_16x16x32_bf16 v[124:127], v[148:151], v[140:143], v[124:127]\n\t" \
  "ds_read_b128 v[192:195], %[a0] offset:16384\n\t" \
  "v_cvt_pk_bf16_f32 v64, v64, v65\n\t" \
  "v_cvt_pk_bf16_f32 v65, v66, v67\n\t" \
  "v_mfma_f32_16x16x32_bf16 v[120:123], v[148:151], v[136:139], v[120:123]\n\t" \
  "ds_read_b128 v[196:199], %[a0] offset:17408\n\t" \
  "v_cvt_pk_bf16_f32 v66, v80, v81\n\t" \
  "v_cvt_pk_bf16_f32 v67, v82, v83\n\t" \
  "v_mfma_f32_16x16x32_bf16 v[116:119], v[148:151], v[132:135], v[116:119]\n\t" \
  "ds_read_b128 v[200:203], %[a0] offset:18432\n\t" \
  "buffer_store_dwordx4 v[64:67], %[vx], %[rs], %[x2] offen offset:0 sc1\n\t" \
  "v_cvt_pk_bf16_f32 v68, v68, v69\n\t" \
  "v_mfma_f32_16x16x32_bf16 v[112:115], v[148:151], v[128:131], v[112:115]\n\t" \
  "ds_read_b128 v[204:207], %[a0] offset:19456\n\t" \
  "v_cvt_pk_bf16_f32 v69, v70, v71\n\t" \
  "v_cvt_pk_bf16_f32 v70, v84, v85\n\t" \
  "s_waitcnt lgkmcnt(4)\n\t" \
  "v_mfma_f32_16x16x32_bf16 v[96:99], v[152:155], v[176:179], v[96:99]\n\t" \
  "s_barrier\n\t" \
  "ds_read_b64_tr_b16 v[144:145], %[b0e0] offset:0\n\t" \
  "v_cvt_pk_bf16_f32 v71, v86, v87\n\t" \
  "buffer_store_dwordx4 v[68:71], %[vx], %[rs], %[x2] offen offset:1024 sc1\n\t" \
  "v_mfma_f32_16x16x32_bf16 v[100:103], v[152:155], v[180:183], v[100:103]\n\t" \
  "ds_read_b64_tr_b16 v[146:147], %[b0e0] offset:2048\n\t" \
  "v_cvt_pk_bf16_f32 v72, v72, v73\n\t" \
  "v_cvt_pk_bf16_f32 v73, v74, v75\n\t" \
  "v_mfma_f32_16x16x32_bf16 v[104:107], v[152:155], v[184:187], v[104:107]\n\t" \
  "ds_read_b64_tr_b16 v[148:149], %[b0e1] offset:0\n\t" \
  "v_cvt_pk_bf16_f32 v74, v88, v89\n\t" \
  "v_cvt_pk_bf16_f32 v75, v90, v91\n\t" \
  "v_mfma_f32_16x16x32_bf16 v[108:111], v[152:155], v[188:191], v[108:111]\n\t" \
  "ds_read_b64_tr_b16 v[150:151], %[b0e1] offset:2048\n\t" \
  "buffer_store_dwordx4 v[72:75], %[vx], %[rs], %[x2] offen offset:2048 sc1\n\t" \
  "v_cvt_pk_bf16_f32 v76, v76, v77\n\t" \
  "v_mfma_f32_16x16x32_bf16 v[124:127], v[156:159], v[188:191], v[124:127]\n\t" \
  "ds_read_b128 v[208:211], %[a0] offset:24576\n\t" \
  "v_cvt_pk_bf16_f32 v77, v78, v79\n\t" \
  "v_cvt_pk_bf16_f32 v78, v92, v93\n\t" \
  "v_mfma_f32_16x16x32_bf16 v[120:123], v[156:159], v[184:187], v[120:123]\n\t" \
  "ds_read_b128 v[212:215], %[a0] offset:25600\n\t" \
  "v_cvt_pk_bf16_f32 v79, v94, v95\n\t" \
  "buffer_store_dwordx4 v[76:79], %[vx], %[rs], %[x2] offen offset:3072 sc1\n\t" \
  "v_mfma_f32_16x16x32_bf16 v[116:119], v[156:159], v[180:183], v[116:119]\n\t" \
  "ds_read_b128 v[216:219], %[a0] offset:26624\n\t" \
  "s_waitcnt vmcnt(4)\n\t" \
  "buffer_store_dwordx2 %[tg], %[vf], %[rs], %[f1] offen sc1\n\t" \
  "v_mfma_f32_16x16x32_bf16 v[112:115], v[156:159], v[176:179], v[112:115]\n\t" \
  "ds_read_b128 v[220:223], %[a0] offset:27648\n\t" \
  "s_waitcnt lgkmcnt(8)\n\t" \
  "v_mfma_f32_16x16x32_bf16 v[96:99], v[160:163], v[192:195], v[96:99]\n\t" \
  "ds_read_b64_tr_b16 v[152:153], %[b0e0] offset:4096\n\t" \
  "v_mfma_f32_16x16x32_bf16 v[100:103], v[160:163], v[196:199], v[100:103]\n\t" \
  "ds_read_b64_tr_b16 v[154:155], %[b0e0] offset:6144\n\t" \
  "v_mfma_f32_16x16x32_bf16 v[104:107], v[160:163], v[200:203], v[104:107]\n\t" \
  "ds_read_b64_tr_b16 v[156:157], %[b0e1] offset:4096\n\t" \
  "v_mfma_f32_16x16x32_bf16 v[108:111], v[160:163], v[204:207], v[108:111]\n\t" \
  "ds_read_b64_tr_b16 v[158:159], %[b0e1] offset:6144\n\t" \
  "v_mfma_f32_16x16x32_bf16 v[124:127], v[164:167], v[204:207], v[124:127]\n\t" \
  "ds_read_b128 v[128:131], %[a0] offset:32768\n\t" \
  "v_mfma_f32_16x16x32_bf16 v[120:123], v[164:167], v[200:203], v[120:123]\n\t" \
  "ds_read_b128 v[132:135], %[a0] offset:33792\n\t" \
  "v_mfma_f32_16x16x32_bf16 v[116:119], v[164:167], v[196:199], v[116:119]\n\t" \
  "ds_read_b128 v[136:139], %[a0] offset:34816\n\t" \
  "v_mfma_f32_16x16x32_bf16 v[112:115], v[164:167], v[192:195], v[112:115]\n\t" \
  "ds_read_b128 v[140:143], %[a0] offset:35840\n\t" \
  "s_waitcnt lgkmcnt(8)\n\t" \
  "v_mfma_f32_16x16x32_bf16 v[96:99], v[168:171], v[208:211], v[96:99]\n\t" \
  "ds_read_b64_tr_b16 v[160:161], %[b0e0] offset:8192\n\t" \
  "v_mfma_f32_16x16x32_bf16 v[100:103], v[168:171], v[212:215], v[100:103]\n\t" \
  "ds_read_b64_tr_b16 v[162:163], %[b0e0] offset:10240\n\t" \
  "v_mfma_f32_16x16x32_bf16 v[104:107], v[168:171], v[216:219], v[104:107]\n\t" \
  "ds_read_b64_tr_b16 v[164:165], %[b0e1] offset:8192\n\t" \
  "v_mfma_f32_16x16x32_bf16 v[108:111], v[168:171], v[220:223], v[108:111]\n\t" \
  "ds_read_b64_tr_b16 v[166:167], %[b0e1] offset:10240\n\t" \
  "v_mfma_f32_16x16x32_bf16 v[124:127], v[172:175], v[220:223], v[124:127]\n\t" \
  "ds_read_b128 v[176:179], %[a0] offset:40960\n\t" \
  "v_mfma_f32_16x16x32_bf16 v[120:123], v[172:175], v[216:219], v[120:123]\n\t" \
  "ds_read_b128 v[180:183], %[a0] offset:41984\n\t" \
  "v_mfma_f32_16x16x32_bf16 v[116:119], v[172:175], v[212:215], v[116:119]\n\t" \
  "ds_read_b128 v[184:187], %[a0] offset:43008\n\t" \
  "v_mfma_f32_16x16x32_bf16 v[112:115], v[172:175], v[208:211], v[112:115]\n\t" \
  "ds_read_b128 v[188:191], %[a0] offset:44032\n\t" \
  "s_waitcnt lgkmcnt(8)\n\t" \
  "v_mfma_f32_16x16x32_bf16 v[96:99], v[144:147], v[128:131], v[96:99]\n\t" \
  "ds_read_b64_tr_b16 v[168:169], %[b0e0] offset:12288\n\t" \
  "v_mfma_f32_16x16x32_bf16 v[100:103], v[144:147], v[132:135], v[100:103]\n\t" \
  "ds_read_b64_tr_b16 v[170:171], %[b0e0] offset:14336\n\t" \
  "v_mfma_f32_16x16x32_bf16 v[104:107], v[144:147], v[136:139], v[104:107]\n\t" \
  "ds_read_b64_tr_b16 v[172:173], %[b0e1] offset:12288\n\t" \
  "v_mfma_f32_16x16x32_bf16 v[108:111], v[144:147], v[140:143], v[108:111]\n\t" \
  "ds_read_b64_tr_b16 v[174:175], %[b0e1] offset:14336\n\t" \
  "v_mfma_f32_16x16x32_bf16 v[124:127], v[148:151], v[140:143], v[124:127]\n\t" \
  "ds_read_b128 v[192:195], %[a0] offset:49152\n\t" \
  "v_mfma_f32_16x16x32_bf16 v[120:123], v[148:151], v[136:139], v[120:123]\n\t" \
  "ds_read_b128 v[196:199], %[a0] offset:50176\n\t" \
  "v_mfma_f32_16x16x32_bf16 v[116:119], v[148:151], v[132:135], v[116:119]\n\t" \
  "ds_read_b128 v[200:203], %[a0] offset:51200\n\t" \
  "v_mfma_f32_16x16x32_bf16 v[112:115], v[148:151], v[128:131], v[112:115]\n\t" \
  "ds_read_b128 v[204:207], %[a0] offset:52224\n\t" \
  "s_waitcnt lgkmcnt(8)\n\t" \
  "v_mfma_f32_16x16x32_bf16 v[96:99], v[152:155], v[176:179], v[96:99]\n\t" \
  "ds_read_b128 v[208:211], %[a0] offset:57344\n\t" \
  "v_mfma_f32_16x16x32_bf16 v[100:103], v[152:155], v[180:183], v[100:103]\n\t" \
  "ds_read_b128 v[212:215], %[a0] offset:58368\n\t" \
  "v_mfma_f32_16x16x32_bf16 v[104:107], v[152:155], v[184:187], v[104:107]\n\t" \
  "ds_read_b128 v[216:219], %[a0] offset:59392\n\t" \
  "v_mfma_f32_16x16x32_bf16 v[108:111], v[152:155], v[188:191], v[108:111]\n\t" \
  "ds_read_b128 v[220:223], %[a0] offset:60416\n\t" \
  "v_mfma_f32_16x16x32_bf16 v[124:127], v[156:159], v[188:191], v[124:127]\n\t" \
  "v_mfma_f32_16x16x32_bf16 v[120:123], v[156:159], v[184:187], v[120:123]\n\t" \
  "v_mfma_f32_16x16x32_bf16 v[116:119], v[156:159], v[180:183], v[116:119]\n\t" \
  "v_mfma_f32_16x16x32_bf16 v[112:115], v[156:159], v[176:179], v[112:115]\n\t" \
  "s_waitcnt lgkmcnt(4)\n\t" \
  "v_mfma_f32_16x16x32_bf16 v[96:99], v[160:163], v[192:195], v[96:99]\n\t" \
  "v_mfma_f32_16x16x32_bf16 v[100:103], v[160:163], v[196:199], v[100:103]\n\t" \
  "v_mfma_f32_16x16x32_bf16 v[104:107], v[160:163], v[200:203], v[104:107]\n\t" \
  "v_mfma_f32_16x16x32_bf16 v[108:111], v[160:163], v[204:207], v[108:111]\n\t" \
  "v_mfma_f32_16x16x32_bf16 v[124:127], v[164:167], v[204:207], v[124:127]\n\t" \
  "v_mfma_f32_16x16x32_bf16 v[120:123], v[164:167], v[200:203], v[120:123]\n\t" \
  "v_mfma_f32_16x16x32_bf16 v[116:119], v[164:167], v[196:199], v[116:119]\n\t" \
  "v_mfma_f32_16x16x32_bf16 v[112:115], v[164:167], v[192:195], v[112:115]\n\t" \
  "s_waitcnt lgkmcnt(0)\n\t" \
  "v_mfma_f32_16x16x32_bf16 v[96:99], v[168:171], v[208:211], v[96:99]\n\t" \
  "v_mfma_f32_16x16x32_bf16 v[100:103], v[168:171], v[212:215], v[100:103]\n\t" \
  "v_mfma_f32_16x16x32_bf16 v[104:107], v[168:171], v[216:219], v[104:107]\n\t" \
  "v_mfma_f32_16x16x32_bf16 v[108:111], v[168:171], v[220:223], v[108:111]\n\t" \
  "v_mfma_f32_16x16x32_bf16 v[124:127], v[172:175], v[220:223], v[124:127]\n\t" \
  "v_mfma_f32_16x16x32_bf16 v[120:123], v[172:175], v[216:219], v[120:123]\n\t" \
  "v_mfma_f32_16x16x32_bf16 v[116:119], v[172:175], v[212:215], v[116:119]\n\t" \
  "v_mfma_f32_16x16x32_bf16 v[112:115], v[172:175], v[208:211], v[112:115]\n\t" \
  "s_nop 7\n\t" \
  ""
#define FF2_CLOB "v0","v1","v2","v3","v4","v5","v6","v7","v8","v9","v10","v11","v12","v13","v14","v15","v16","v17","v18","v19","v20","v21","v22","v23","v24","v25","v26","v27","v28","v29","v30","v31","v32","v33","v34","v35","v36","v37","v38","v39","v40","v41","v42","v43","v44","v45","v46","v47","v48","v49","v50","v51","v52","v53","v54","v55","v56","v57","v58","v59","v60","v61","v62","v63","v64","v65","v66","v67","v68","v69","v70","v71","v72","v73","v74","v75","v76","v77","v78","v79","v80","v81","v82","v83","v84","v85","v86","v87","v88","v89","v90","v91","v92","v93","v94","v95","v128","v129","v130","v131","v132","v133","v134","v135","v136","v137","v138","v139","v140","v141","v142","v143","v144","v145","v146","v147","v148","v149","v150","v151","v152","v153","v154","v155","v156","v157","v158","v159","v160","v161","v162","v163","v164","v165","v166","v167","v168","v169","v170","v171","v172","v173","v174","v175","v176","v177","v178","v179","v180","v181","v182","v183","v184","v185","v186","v187","v188","v189","v190","v191","v192","v193","v194","v195","v196","v197","v198","v199","v200","v201","v202","v203","v204","v205","v206","v207","v208","v209","v210","v211","v212","v213","v214","v215","v216","v217","v218","v219","v220","v221","v222","v223"
#define WO_ASM \
  "s_add_i32 %[t], %[so], %[rot]\n\t" \
  "buffer_load_dwordx4 v[64:67], %[vo], %[rs], %[t] offen sc1\n\t" \
  "s_xor_b32 %[t], %[t], 1024\n\t" \
  "buffer_load_dwordx4 v[68:71], %[vo], %[rs], %[t] offen sc1\n\t" \
  "s_add_i32 %[rot], %[rot], 8192\n\t" \
  "s_and_b32 %[rot], %[rot], 262143\n\t" \
  "s_add_i32 %[t], %[so], %[rot]\n\t" \
  "buffer_load_dwordx4 v[72:75], %[vo], %[rs], %[t] offen sc1\n\t" \
  "s_xor_b32 %[t], %[t], 1024\n\t" \
  "buffer_load_dwordx4 v[76:79], %[vo], %[rs], %[t] offen sc1\n\t" \
  "s_add_i32 %[rot], %[rot], 8192\n\t" \
  "s_and_b32 %[rot], %[rot], 262143\n\t" \
  "s_add_i32 %[t], %[so], %[rot]\n\t" \
  "buffer_load_dwordx4 v[80:83], %[vo], %[rs], %[t] offen sc1\n\t" \
  "s_xor_b32 %[t], %[t], 1024\n\t" \
  "buffer_load_dwordx4 v[84:87], %[vo], %[rs], %[t] offen sc1\n\t" \
  "s_add_i32 %[rot], %[rot], 8192\n\t" \
  "s_and_b32 %[rot], %[rot], 262143\n\t" \
  "v_mov_b64 v[0:1], 0\n\t" \
  "v_mov_b64 v[2:3], 0\n\t" \
  "v_mov_b64 v[4:5], 0\n\t" \
  "v_mov_b64 v[6:7], 0\n\t" \
  "v_mov_b64 v[8:9], 0\n\t" \
  "v_mov_b64 v[10:11], 0\n\t" \
  "v_mov_b64 v[12:13], 0\n\t" \
  "v_mov_b64 v[14:15], 0\n\t" \
  "v_mov_b64 v[16:17], 0\n\t" \
  "v_mov_b64 v[18:19], 0\n\t" \
  "v_mov_b64 v[20:21], 0\n\t" \
  "v_mov_b64 v[22:23], 0\n\t" \
  "v_mov_b64 v[24:25], 0\n\t" \
  "v_mov_b64 v[26:27], 0\n\t" \
  "v_mov_b64 v[28:29], 0\n\t" \
  "v_mov_b64 v[30:31], 0\n\t" \
  "ds_read_b64_tr_b16 v[32:33], %[e0] offset:0\n\t" \
  "ds_read_b64_tr_b16 v[34:35], %[e0] offset:2048\n\t" \
  "ds_read_b64_tr_b16 v[36:37], %[e1] offset:0\n\t" \
  "ds_read_b64_tr_b16 v[38:39], %[e1] offset:2048\n\t" \
  "ds_read_b64_tr_b16 v[40:41], %[e0] offset:512\n\t" \
  "ds_read_b64_tr_b16 v[42:43], %[e0] offset:2560\n\t" \
  "ds_read_b64_tr_b16 v[44:45], %[e1] offset:512\n\t" \
  "ds_read_b64_tr_b16 v[46:47], %[e1] offset:2560\n\t" \
  "s_mov_b32 %[cnt], 3\n\t" \
  "1:\n\t" \
  "s_add_i32 %[t], %[so], %[rot]\n\t" \
  "buffer_load_dwordx4 v[88:91], %[vo], %[rs], %[t] offen sc1\n\t" \
  "s_xor_b32 %[t], %[t], 1024\n\t" \
  "buffer_load_dwordx4 v[92:95], %[vo], %[rs], %[t] offen sc1\n\t" \
  "s_add_i32 %[rot], %[rot], 8192\n\t" \
  "s_and_b32 %[rot], %[rot], 262143\n\t" \
  "s_waitcnt vmcnt(6) lgkmcnt(4)\n\t" \
  "v_mfma_f32_16x16x32_bf16 v[0:3], v[32:35], v[64:67], v[0:3]\n\t" \
  "ds_read_b64_tr_b16 v[48:49], %[e0] offset:4096\n\t" \
  "v_mfma_f32_16x16x32_bf16 v[16:19], v[32:35], v[68:71], v[16:19]\n\t" \
  "ds_read_b64_tr_b16 v[50:51], %[e0] offset:6144\n\t" \
  "v_mfma_f32_16x16x32_bf16 v[20:23], v[36:39], v[68:71], v[20:23]\n\t" \
  "ds_read_b64_tr_b16 v[52:53], %[e1] offset:4096\n\t" \
  "v_mfma_f32_16x16x32_bf16 v[4:7], v[36:39], v[64:67], v[4:7]\n\t" \
  "ds_read_b64_tr_b16 v[54:55], %[e1] offset:6144\n\t" \
  "s_waitcnt lgkmcnt(4)\n\t" \
  "v_mfma_f32_16x16x32_bf16 v[8:11], v[40:43], v[64:67], v[8:11]\n\t" \
  "ds_read_b64_tr_b16 v[56:57], %[e0] offset:4608\n\t" \
  "v_mfma_f32_16x16x32_bf16 v[24:27], v[40:43], v[68:71], v[24:27]\n\t" \
  "ds_read_b64_tr_b16 v[58:59], %[e0] offset:6656\n\t" \
  "v_mfma_f32_16x16x32_bf16 v[28:31], v[44:47], v[68:71], v[28:31]\n\t" \
  "ds_read_b64_tr_b16 v[60:61], %[e1] offset:4608\n\t" \
  "v_mfma_f32_16x16x32_bf16 v[12:15], v[44:47], v[64:67], v[12:15]\n\t" \
  "ds_read_b64_tr_b16 v[62:63], %[e1] offset:6656\n\t" \
  "s_add_i32 %[t], %[so], %[rot]\n\t" \
  "buffer_load_dwordx4 v[64:67], %[vo], %[rs], %[t] offen sc1\n\t" \
  "s_xor_b32 %[t], %[t], 1024\n\t" \
  "buffer_load_dwordx4 v[68:71], %[vo], %[rs], %[t] offen sc1\n\t" \
  "s_add_i32 %[rot], %[rot], 8192\n\t" \
  "s_and_b32 %[rot], %[rot], 262143\n\t" \
  "s_waitcnt vmcnt(6) lgkmcnt(4)\n\t" \
  "v_mfma_f32_16x16x32_bf16 v[0:3], v[48:51], v[72:75], v[0:3]\n\t" \
  "ds_read_b64_tr_b16 v[32:33], %[e0] offset:8192\n\t" \
  "v_mfma_f32_16x16x32_bf16 v[16:19], v[48:51], v[76:79], v[16:19]\n\t" \
  "ds_read_b64_tr_b16 v[34:35], %[e0] offset:10240\n\t" \
  "v_mfma_f32_16x16x32_bf16 v[20:23], v[52:55], v[76:79], v[20:23]\n\t" \
  "ds_read_b64_tr_b16 v[36:37], %[e1] offset:8192\n\t" \
  "v_mfma_f32_16x16x32_bf16 v[4:7], v[52:55], v[72:75], v[4:7]\n\t" \
  "ds_read_b64_tr_b16 v[38:39], %[e1] offset:10240\n\t" \
  "s_waitcnt lgkmcnt(4)\n\t" \
  "v_mfma_f32_16x16x32_bf16 v[8:11], v[56:59], v[72:75], v[8:11]\n\t" \
  "ds_read_b64_tr_b16 v[40:41], %[e0] offset:8704\n\t" \
  "v_mfma_f32_16x16x32_bf16 v[24:27], v[56:59], v[76:79], v[24:27]\n\t" \
  "ds_read_b64_tr_b16 v[42:43], %[e0] offset:10752\n\t" \
  "v_mfma_f32_16x16x32_bf16 v[28:31], v[60:63], v[76:79], v[28:31]\n\t" \
  "ds_read_b64_tr_b16 v[44:45], %[e1] offset:8704\n\t" \
  "v_mfma_f32_16x16x32_bf16 v[12:15], v[60:63], v[72:75], v[12:15]\n\t" \
  "ds_read_b64_tr_b16 v[46:47], %[e1] offset:10752\n\t" \
  "s_add_i32 %[t], %[so], %[rot]\n\t" \
  "buffer_load_dwordx4 v[72:75], %[vo], %[rs], %[t] offen sc1\n\t" \
  "s_xor_b32 %[t], %[t], 1024\n\t" \
  "buffer_load_dwordx4 v[76:79], %[vo], %[rs], %[t] offen sc1\n\t" \
  "s_add_i32 %[rot], %[rot], 8192\n\t" \
  "s_and_b32 %[rot], %[rot], 262143\n\t" \
  "s_waitcnt vmcnt(6) lgkmcnt(4)\n\t" \
  "v_mfma_f32_16x16x32_bf16 v[0:3], v[32:35], v[80:83], v[0:3]\n\t" \
  "ds_read_b64_tr_b16 v[48:49], %[e0] offset:12288\n\t" \
  "v_mfma_f32_16x16x32_bf16 v[16:19], v[32:35], v[84:87], v[16:19]\n\t" \
  "ds_read_b64_tr_b16 v[50:51], %[e0] offset:14336\n\t" \
  "v_mfma_f32_16x16x32_bf16 v[20:23], v[36:39], v[84:87], v[20:23]\n\t" \
  "ds_read_b64_tr_b16 v[52:53], %[e1] offset:12288\n\t" \
  "v_mfma_f32_16x16x32_bf16 v[4:7], v[36:39], v[80:83], v[4:7]\n\t" \
  "ds_read_b64_tr_b16 v[54:55], %[e1] offset:14336\n\t" \
  "s_waitcnt lgkmcnt(4)\n\t" \
  "v_mfma_f32_16x16x32_bf16 v[8:11], v[40:43], v[80:83], v[8:11]\n\t" \
  "ds_read_b64_tr_b16 v[56:57], %[e0] offset:12800\n\t" \
  "v_mfma_f32_16x16x32_bf16 v[24:27], v[40:43], v[84:87], v[24:27]\n\t" \
  "ds_read_b64_tr_b16 v[58:59], %[e0] offset:14848\n\t" \
  "v_mfma_f32_16x16x32_bf16 v[28:31], v[44:47], v[84:87], v[28:31]\n\t" \
  "ds_read_b64_tr_b16 v[60:61], %[e1] offset:12800\n\t" \
  "v_mfma_f32_16x16x32_bf16 v[12:15], v[44:47], v[80:83], v[12:15]\n\t" \
  "ds_read_b64_tr_b16 v[62:63], %[e1] offset:14848\n\t" \
  "s_add_i32 %[t], %[so], %[rot]\n\t" \
  "buffer_load_dwordx4 v[80:83], %[vo], %[rs], %[t] offen sc1\n\t" \
  "s_xor_b32 %[t], %[t], 1024\n\t" \
  "buffer_load_dwordx4 v[84:87], %[vo], %[rs], %[t] offen sc1\n\t" \
  "s_add_i32 %[rot], %[rot], 8192\n\t" \
  "s_and_b32 %[rot], %[rot], 262143\n\t" \
  "s_waitcnt vmcnt(6) lgkmcnt(4)\n\t" \
  "v_mfma_f32_16x16x32_bf16 v[0:3], v[48:51], v[88:91], v[0:3]\n\t" \
  "ds_read_b64_tr_b16 v[32:33], %[e0] offset:16384\n\t" \
  "v_mfma_f32_16x16x32_bf16 v[16:19], v[48:51], v[92:95], v[16:19]\n\t" \
  "ds_read_b64_tr_b16 v[34:35], %[e0] offset:18432\n\t" \
  "v_mfma_f32_16x16x32_bf16 v[20:23], v[52:55], v[92:95], v[20:23]\n\t" \
  "ds_read_b64_tr_b16 v[36:37], %[e1] offset:16384\n\t" \
  "v_mfma_f32_16x16x32_bf16 v[4:7], v[52:55], v[88:91], v[4:7]\n\t" \
  "ds_read_b64_tr_b16 v[38:39], %[e1] offset:18432\n\t" \
  "s_waitcnt lgkmcnt(4)\n\t" \
  "v_mfma_f32_16x16x32_bf16 v[8:11], v[56:59], v[88:91], v[8:11]\n\t" \
  "ds_read_b64_tr_b16 v[40:41], %[e0] offset:16896\n\t" \
  "v_mfma_f32_16x16x32_bf16 v[24:27], v[56:59], v[92:95], v[24:27]\n\t" \
  "ds_read_b64_tr_b16 v[42:43], %[e0] offset:18944\n\t" \
  "v_mfma_f32_16x16x32_bf16 v[28:31], v[60:63], v[92:95], v[28:31]\n\t" \
  "ds_read_b64_tr_b16 v[44:45], %[e1] offset:16896\n\t" \
  "v_mfma_f32_16x16x32_bf16 v[12:15], v[60:63], v[88:91], v[12:15]\n\t" \
  "ds_read_b64_tr_b16 v[46:47], %[e1] offset:18944\n\t" \
  "v_add_u32 %[e0], 16384, %[e0]\n\t" \
  "v_add_u32 %[e1], 16384, %[e1]\n\t" \
  "s_add_i32 %[cnt], %[cnt], -1\n\t" \
  "s_cmp_lg_u32 %[cnt], 0\n\t" \
  "s_cbranch_scc1 1b\n\t" \
  "s_add_i32 %[t], %[so], %[rot]\n\t" \
  "buffer_load_dwordx4 v[88:91], %[vo], %[rs], %[t] offen sc1\n\t" \
  "s_xor_b32 %[t], %[t], 1024\n\t" \
  "buffer_load_dwordx4 v[92:95], %[vo], %[rs], %[t] offen sc1\n\t" \
  "s_barrier\n\t" \
  "s_add_i32 m0, %[l0], 0\n\t" \
  "s_nop 0\n\t" \
  "buffer_load_dwordx4 %[w0], %[rw], %[s0] offen lds\n\t" \
  "s_add_i32 m0, %[l1], 0\n\t" \
  "s_nop 0\n\t" \
  "buffer_load_dwordx4 %[w1], %[rw], %[s0] offen lds\n\t" \
  "s_add_i32 m0, %[l0], -14336\n\t" \
  "s_nop 0\n\t" \
  "buffer_load_dwordx4 %[w0], %[rw], %[s1] offen lds\n\t" \
  "s_add_i32 m0, %[l1], -14336\n\t" \
  "s_nop 0\n\t" \
  "buffer_load_dwordx4 %[w1], %[rw], %[s1] offen lds\n\t" \
  "s_waitcnt vmcnt(10) lgkmcnt(4)\n\t" \
  "v_mfma_f32_16x16x32_bf16 v[0:3], v[32:35], v[64:67], v[0:3]\n\t" \
  "ds_read_b64_tr_b16 v[48:49], %[e0] offset:4096\n\t" \
  "v_mfma_f32_16x16x32_bf16 v[16:19], v[32:35], v[68:71], v[16:19]\n\t" \
  "ds_read_b64_tr_b16 v[50:51], %[e0] offset:6144\n\t" \
  "v_mfma_f32_16x16x32_bf16 v[20:23], v[36:39], v[68:71], v[20:23]\n\t" \
  "ds_read_b64_tr_b16 v[52:53], %[e1] offset:4096\n\t" \
  "v_mfma_f32_16x16x32_bf16 v[4:7], v[36:39], v[64:67], v[4:7]\n\t" \
  "ds_read_b64_tr_b16 v[54:55], %[e1] offset:6144\n\t" \
  "s_waitcnt lgkmcnt(4)\n\t" \
  "v_mfma_f32_16x16x32_bf16 v[8:11], v[40:43], v[64:67], v[8:11]\n\t" \
  "ds_read_b64_tr_b16 v[56:57], %[e0] offset:4608\n\t" \
  "v_mfma_f32_16x16x32_bf16 v[24:27], v[40:43], v[68:71], v[24:27]\n\t" \
  "ds_read_b64_tr_b16 v[58:59], %[e0] offset:6656\n\t" \
  "v_mfma_f32_16x16x32_bf16 v[28:31], v[44:47], v[68:71], v[28:31]\n\t" \
  "ds_read_b64_tr_b16 v[60:61], %[e1] offset:4608\n\t" \
  "v_mfma_f32_16x16x32_bf16 v[12:15], v[44:47], v[64:67], v[12:15]\n\t" \
  "ds_read_b64_tr_b16 v[62:63], %[e1] offset:6656\n\t" \
  "s_waitcnt vmcnt(8) lgkmcnt(4)\n\t" \
  "v_mfma_f32_16x16x32_bf16 v[0:3], v[48:51], v[72:75], v[0:3]\n\t" \
  "ds_read_b64_tr_b16 v[32:33], %[e0] offset:8192\n\t" \
  "v_mfma_f32_16x16x32_bf16 v[16:19], v[48:51], v[76:79], v[16:19]\n\t" \
  "ds_read_b64_tr_b16 v[34:35], %[e0] offset:10240\n\t" \
  "v_mfma_f32_16x16x32_bf16 v[20:23], v[52:55], v[76:79], v[20:23]\n\t" \
  "ds_read_b64_tr_b16 v[36:37], %[e1] offset:8192\n\t" \
  "v_mfma_f32_16x16x32_bf16 v[4:7], v[52:55], v[72:75], v[4:7]\n\t" \
  "ds_read_b64_tr_b16 v[38:39], %[e1] offset:10240\n\t" \
  "s_waitcnt lgkmcnt(4)\n\t" \
  "v_mfma_f32_16x16x32_bf16 v[8:11], v[56:59], v[72:75], v[8:11]\n\t" \
  "ds_read_b64_tr_b16 v[40:41], %[e0] offset:8704\n\t" \
  "v_mfma_f32_16x16x32_bf16 v[24:27], v[56:59], v[76:79], v[24:27]\n\t" \
  "ds_read_b64_tr_b16 v[42:43], %[e0] offset:10752\n\t" \
  "v_mfma_f32_16x16x32_bf16 v[28:31], v[60:63], v[76:79], v[28:31]\n\t" \
  "ds_read_b64_tr_b16 v[44:45], %[e1] offset:8704\n\t" \
  "v_mfma_f32_16x16x32_bf16 v[12:15], v[60:63], v[72:75], v[12:15]\n\t" \
  "ds_read_b64_tr_b16 v[46:47], %[e1] offset:10752\n\t" \
  "s_waitcnt vmcnt(6) lgkmcnt(4)\n\t" \
  "v_mfma_f32_16x16x32_bf16 v[0:3], v[32:35], v[80:83], v[0:3]\n\t" \
  "ds_read_b64_tr_b16 v[48:49], %[e0] offset:12288\n\t" \
  "v_mfma_f32_16x16x32_bf16 v[16:19], v[32:35], v[84:87], v[16:19]\n\t" \
  "ds_read_b64_tr_b16 v[50:51], %[e0] offset:14336\n\t" \
  "v_mfma_f32_16x16x32_bf16 v[20:23], v[36:39], v[84:87], v[20:23]\n\t" \
  "ds_read_b64_tr_b16 v[52:53], %[e1] offset:12288\n\t" \
  "v_mfma_f32_16x16x32_bf16 v[4:7], v[36:39], v[80:83], v[4:7]\n\t" \
  "ds_read_b64_tr_b16 v[54:55], %[e1] offset:14336\n\t" \
  "s_waitcnt lgkmcnt(4)\n\t" \
  "v_mfma_f32_16x16x32_bf16 v[8:11], v[40:43], v[80:83], v[8:11]\n\t" \
  "ds_read_b64_tr_b16 v[56:57], %[e0] offset:12800\n\t" \
  "v_mfma_f32_16x16x32_bf16 v[24:27], v[40:43], v[84:87], v[24:27]\n\t" \
  "ds_read_b64_tr_b16 v[58:59], %[e0] offset:14848\n\t" \
  "v_mfma_f32_16x16x32_bf16 v[28:31], v[44:47], v[84:87], v[28:31]\n\t" \
  "ds_read_b64_tr_b16 v[60:61], %[e1] offset:12800\n\t" \
  "v_mfma_f32_16x16x32_bf16 v[12:15], v[44:47], v[80:83], v[12:15]\n\t" \
  "ds_read_b64_tr_b16 v[62:63], %[e1] offset:14848\n\t" \
  "s_waitcnt vmcnt(4) lgkmcnt(4)\n\t" \
  "v_mfma_f32_16x16x32_bf16 v[0:3], v[48:51], v[88:91], v[0:3]\n\t" \
  "v_mfma_f32_16x16x32_bf16 v[16:19], v[48:51], v[92:95], v[16:19]\n\t" \
  "v_mfma_f32_16x16x32_bf16 v[20:23], v[52:55], v[92:95], v[20:23]\n\t" \
  "v_mfma_f32_16x16x32_bf16 v[4:7], v[52:55], v[88:91], v[4:7]\n\t" \
  "s_waitcnt lgkmcnt(0)\n\t" \
  "v_mfma_f32_16x16x32_bf16 v[8:11], v[56:59], v[88:91], v[8:11]\n\t" \
  "v_mfma_f32_16x16x32_bf16 v[24:27], v[56:59], v[92:95], v[24:27]\n\t" \
  "v_mfma_f32_16x16x32_bf16 v[28:31], v[60:63], v[92:95], v[28:31]\n\t" \
  "v_mfma_f32_16x16x32_bf16 v[12:15], v[60:63], v[88:91], v[12:15]\n\t" \
  "s_nop 7\n\t" \
  ""
#define WO_CLOB "v32","v33","v34","v35","v36","v37","v38","v39","v40","v41","v42","v43","v44","v45","v46","v47","v48","v49","v50","v51","v52","v53","v54","v55","v56","v57","v58","v59","v60","v61","v62","v63","v64","v65","v66","v67","v68","v69","v70","v71","v72","v73","v74","v75","v76","v77","v78","v79","v80","v81","v82","v83","v84","v85","v86","v87","v88","v89","v90","v91","v92","v93","v94","v95"
#define LDA1 \
  "s_nop 0\n\t" \
  "v_readlane_b32 %[so], %[ts], %[s]\n\t" \
  "v_readlane_b32 %[l], %[tl], %[s]\n\t" \
  "s_nop 2\n\t" \
  "s_mov_b32 m0, %[l]\n\t" \
  "s_barrier\n\t" \
  "buffer_load_dwordx4 %[v0], %[rs], %[so] offen sc1 lds\n\t" \
  "1:\n\t" \
  "s_mov_b32 %[t], %[so]\n\t" \
  "s_add_i32 %[s], %[s], 1\n\t" \
  "v_readlane_b32 %[so], %[ts], %[s]\n\t" \
  "v_readlane_b32 %[l], %[tl], %[s]\n\t" \
  "buffer_load_dwordx4 %[v0], %[rs], %[t] offen offset:1024 sc1 lds\n\t" \
  "buffer_load_dwordx4 %[v0], %[rs], %[t] offen offset:2048 sc1 lds\n\t" \
  "buffer_load_dwordx4 %[v0], %[rs], %[t] offen offset:3072 sc1 lds\n\t" \
  "s_cmp_lt_u32 %[s], %[e]\n\t" \
  "s_mov_b32 m0, %[l]\n\t" \
  "s_waitcnt vmcnt(8)\n\t" \
  "s_cbranch_scc0 2f\n\t" \
  "s_barrier\n\t" \
  "buffer_load_dwordx4 %[v0], %[rs], %[so] offen sc1 lds\n\t" \
  "s_branch 1b\n\t" \
  "2:\n\t" \
  ""
#define LDBQ \
  "s_nop 0\n\t" \
  "v_readlane_b32 %[so], %[ts], %[s]\n\t" \
  "v_readlane_b32 %[l], %[tl], %[s]\n\t" \
  "s_nop 2\n\t" \
  "s_mov_b32 m0, %[l]\n\t" \
  "s_barrier\n\t" \
  "buffer_load_dwordx4 %[v0], %[rs], %[so] offen lds\n\t" \
  "1:\n\t" \
  "buffer_load_dwordx4 %[v1], %[rs], %[so] offen offset:1024 lds\n\t" \
  "s_add_i32 m0, %[l], 4096\n\t" \
  "s_add_i32 %[t], %[so], %[sh]\n\t" \
  "buffer_load_dwordx4 %[v0], %[rs], %[t] offen lds\n\t" \
  "buffer_load_dwordx4 %[v1], %[rs], %[t] offen offset:1024 lds\n\t" \
  "s_add_i32 m0, %[l], 8192\n\t" \
  "s_add_i32 %[t], %[t], 2048\n\t" \
  "s_add_i32 %[s], %[s], 1\n\t" \
  "v_readlane_b32 %[so], %[ts], %[s]\n\t" \
  "v_readlane_b32 %[l], %[tl], %[s]\n\t" \
  "buffer_load_dwordx4 %[v0], %[rs], %[t] offen lds\n\t" \
  "buffer_load_dwordx4 %[v1], %[rs], %[t] offen offset:1024 lds\n\t" \
  "s_cmp_lt_u32 %[s], %[e]\n\t" \
  "s_mov_b32 m0, %[l]\n\t" \
  "s_waitcnt vmcnt(36)\n\t" \
  "s_cbranch_scc0 2f\n\t" \
  "s_barrier\n\t" \
  "buffer_load_dwordx4 %[v0], %[rs], %[so] offen lds\n\t" \
  "s_branch 1b\n\t" \
  "2:\n\t" \
  ""
#define LDB1 \
  "s_nop 0\n\t" \
  "v_readlane_b32 %[so], %[ts], %[s]\n\t" \
  "v_readlane_b32 %[l], %[tl], %[s]\n\t" \
  "s_nop 2\n\t" \
  "s_mov_b32 m0, %[l]\n\t" \
  "s_barrier\n\t" \
  "buffer_load_dwordx4 %[v0], %[rs], %[so] offen lds\n\t" \
  "1:\n\t" \
  "buffer_load_dwordx4 %[v1], %[rs], %[so] offen offset:1024 lds\n\t" \
  "s_add_i32 m0, %[l], %[sh]\n\t" \
  "s_nop 0\n\t" \
  "buffer_load_dwordx4 %[vh], %[rs], %[so] offen lds\n\t" \
  "s_add_i32 m0, %[l], 4096\n\t" \
  "s_add_i32 %[t], %[so], 128\n\t" \
  "buffer_load_dwordx4 %[v0], %[rs], %[t] offen lds\n\t" \
  "buffer_load_dwordx4 %[v1], %[rs], %[t] offen offset:1024 lds\n\t" \
  "s_add_i32 m0, %[l], 8192\n\t" \
  "s_add_i32 %[t], %[so], 256\n\t" \
  "s_add_i32 %[s], %[s], 1\n\t" \
  "v_readlane_b32 %[so], %[ts], %[s]\n\t" \
  "v_readlane_b32 %[l], %[tl], %[s]\n\t" \
  "buffer_load_dwordx4 %[v0], %[rs], %[t] offen lds\n\t" \
  "buffer_load_dwordx4 %[v1], %[rs], %[t] offen offset:1024 lds\n\t" \
  "s_cmp_lt_u32 %[s], %[e]\n\t" \
  "s_mov_b32 m0, %[l]\n\t" \
  "s_waitcnt vmcnt(28)\n\t" \
  "s_cbranch_scc0 2f\n\t" \
  "s_barrier\n\t" \
  "buffer_load_dwordx4 %[v0], %[rs], %[so] offen lds\n\t" \
  "s_branch 1b\n\t" \
  "2:\n\t" \
  ""
#define LDB2 \
  "s_nop 0\n\t" \
  "v_readlane_b32 %[so], %[ts], %[s]\n\t" \
  "v_readlane_b32 %[l], %[tl], %[s]\n\t" \
  "s_nop 2\n\t" \
  "s_mov_b32 m0, %[l]\n\t" \
  "s_barrier\n\t" \
  "buffer_load_dwordx4 %[v0], %[rs], %[so] offen lds\n\t" \
  "1:\n\t" \
  "buffer_load_dwordx4 %[v1], %[rs], %[so] offen offset:1024 lds\n\t" \
  "s_add_i32 m0, %[l], 4096\n\t" \
  "s_add_i32 %[t], %[so], %[d1]\n\t" \
  "buffer_load_dwordx4 %[v0], %[rs], %[t] offen lds\n\t" \
  "buffer_load_dwordx4 %[v1], %[rs], %[t] offen offset:1024 lds\n\t" \
  "s_add_i32 m0, %[l], 8192\n\t" \
  "s_add_i32 %[t], %[so], %[d2]\n\t" \
  "buffer_load_dwordx4 %[v0], %[rs], %[t] offen lds\n\t" \
  "buffer_load_dwordx4 %[v1], %[rs], %[t] offen offset:1024 lds\n\t" \
  "s_add_i32 m0, %[l], 12288\n\t" \
  "s_add_i32 %[t], %[so], %[d3]\n\t" \
  "s_add_i32 %[s], %[s], 1\n\t" \
  "v_readlane_b32 %[so], %[ts], %[s]\n\t" \
  "v_readlane_b32 %[l], %[tl], %[s]\n\t" \
  "buffer_load_dwordx4 %[v0], %[rs], %[t] offen lds\n\t" \
  "buffer_load_dwordx4 %[v1], %[rs], %[t] offen offset:1024 lds\n\t" \
  "s_cmp_lt_u32 %[s], %[e]\n\t" \
  "s_mov_b32 m0, %[l]\n\t" \
  "s_waitcnt vmcnt(16)\n\t" \
  "s_cbranch_scc0 2f\n\t" \
  "s_barrier\n\t" \
  "buffer_load_dwordx4 %[v0], %[rs], %[so] offen lds\n\t" \
  "s_branch 1b\n\t" \
  "2:\n\t" \
  ""


constexpr int QM0=7, QM1=8, QM2=0, SLP1=10, SLP2=8, SLP6=8, CLEN=44032;
constexpr unsigned WS_E=0, WS_FH=4096, WS_FC=8192, WS_FO=12288, WS_FH2=16384, WS_FH1=20480;
constexpr unsigned WS_G1=131072, WS_MODP=393216, WS_ST2=655360, WS_KZ=917504, WS_G2=1048576, WS_KVS=1572864, WS_FX=1703936;
constexpr unsigned WS_KVP=2u<<20, WS_H=4u<<20, WS_O=8u<<20, WS_H2=12u<<20, WS_XP=16u<<20, WS_H1=28u<<20, WS_SIZE=45u<<20;
constexpr int SPIN_MAX=200000;
constexpr unsigned OOB=0x80000000u;

DI u32x4 mkrs(u64 a,unsigned n){ u32x4 r={(unsigned)a,(unsigned)(a>>32)&0xffffu,n,0x00020000u}; return r; }
DI rsrc_t mkr(u64 a,unsigned n){ return __builtin_amdgcn_make_buffer_rsrc((void*)a,0,n,0x00020000); }
DI void dma1(u32x4 rs,unsigned lds,unsigned voff,unsigned soff){
  asm volatile("s_mov_b32 m0, %0\n\ts_nop 0\n\tbuffer_load_dwordx4 %1, %2, %3 offen lds"::"s"(lds),"v"(voff),"s"(rs),"s"(soff):"memory"); }
DI void dma4(u32x4 rs,unsigned lds,unsigned voff,unsigned soff){
  asm volatile("s_mov_b32 m0, %0\n\ts_nop 0\n\tbuffer_load_dwordx4 %1, %2, %3 offen sc1 lds\n\tbuffer_load_dwordx4 %1, %2, %3 offen offset:1024 sc1 lds\n\t"
               "buffer_load_dwordx4 %1, %2, %3 offen offset:2048 sc1 lds\n\tbuffer_load_dwordx4 %1, %2, %3 offen offset:3072 sc1 lds"::"s"(lds),"v"(voff),"s"(rs),"s"(soff):"memory"); }
DI void touch(u32x4 rs,unsigned lds,unsigned voff,unsigned soff){
  asm volatile("s_mov_b32 m0, %0\n\ts_nop 0\n\tbuffer_load_dword %1, %2, %3 offen lds"::"s"(lds),"v"(voff),"s"(rs),"s"(soff):"memory"); }
template<int N> DI void waitvm(){ asm volatile("s_waitcnt vmcnt(%0)"::"n"(N):"memory"); }
DI void bar(){ asm volatile("s_barrier":::"memory"); }
DI void lbar(){ asm volatile("s_waitcnt lgkmcnt(0)\n\ts_barrier":::"memory"); }
DI void fence(){ asm volatile("":::"memory"); }
DI bf16x8 lds128(unsigned a){ return *(LAS bf16x8*)a; }
DI bf16x8 ldstr(unsigned a0,unsigned a1){
  s16x4 lo=__builtin_amdgcn_ds_read_tr16_b64_v4i16((LAS s16x4*)a0), hi=__builtin_amdgcn_ds_read_tr16_b64_v4i16((LAS s16x4*)a1);
  s16x8 v=__builtin_shufflevector(lo,hi,0,1,2,3,4,5,6,7); return __builtin_bit_cast(bf16x8,v); }
DI unsigned pk2(f32x2 v){ bf16x2 r=__builtin_convertvector(v,bf16x2); return __builtin_bit_cast(unsigned,r); }
DI unsigned pk2(float a,float b){ f32x2 v={a,b}; return pk2(v); }
DI float blo(unsigned x){ return __uint_as_float(x<<16); }
DI float bhi(unsigned x){ return __uint_as_float(x&0xffff0000u); }
DI f32x2 up2(unsigned x){ f32x2 v={blo(x),bhi(x)}; return v; }
DI f32x2 fma2(f32x2 a,f32x2 b,f32x2 c){ return __builtin_elementwise_fma(a,b,c); }
DI f32x2 half2(f32x4 v,int i){ f32x2 r={v[2*i],v[2*i+1]}; return r; }
DI float rbf(float a){ return blo(pk2(a,a)); }
DI bf16x8 asbf(u32x4 v){ return __builtin_bit_cast(bf16x8,v); }
DI float xh_add(float x){ unsigned u=__float_as_uint(x); auto s=__builtin_amdgcn_permlane32_swap(u,u,false,false); return __uint_as_float(s[0])+__uint_as_float(s[1]); }
DI float xg_add(float x){ unsigned u=__float_as_uint(x); auto s=__builtin_amdgcn_permlane16_swap(u,u,false,false); return __uint_as_float(s[0])+__uint_as_float(s[1]); }
template<int CTRL> DI float dpp_add(float x){ return x+__int_as_float(__builtin_amdgcn_update_dpp(0,__float_as_int(x),CTRL,0xf,0xf,true)); }
DI float row_sum(float x){ x=dpp_add<0x128>(x); x=dpp_add<0x124>(x); x=dpp_add<0x122>(x); x=dpp_add<0x121>(x); return x; }
#define MFMA16(a,b,c) __builtin_amdgcn_mfma_f32_16x16x32_bf16((a),(b),(c),0,0,0)
#define MFMA32(a,b,c) __builtin_amdgcn_mfma_f32_32x32x16_bf16((a),(b),(c),0,0,0)
DI void st128(rsrc_t r,u32x4 v,unsigned voff,unsigned soff){ asm("":"+v"(v)); __builtin_amdgcn_raw_buffer_store_b128(v,r,voff,soff,16); asm volatile("s_nop 1"::"v"(v):"memory"); }
DI void st64(rsrc_t r,u32x2 v,unsigned voff,unsigned soff){ __builtin_amdgcn_raw_buffer_store_b64(v,r,voff,soff,16); }
DI u32x4 ld128(rsrc_t r,unsigned voff,unsigned soff){ return __builtin_amdgcn_raw_buffer_load_b128(r,voff,soff,16); }
DI u32x2 ld64(rsrc_t r,unsigned voff,unsigned soff){ return __builtin_amdgcn_raw_buffer_load_b64(r,voff,soff,16); }
DI bool all_set(u32x2 f,u32x2 tag){ return __builtin_amdgcn_ballot_w64(__builtin_bit_cast(u64,f)!=__builtin_bit_cast(u64,tag))==0; }
DI bool none(bool c){ return __builtin_amdgcn_ballot_w64(c)==0; }
DI void wait_flags(rsrc_t r,unsigned off,unsigned voff,u32x2 tag){
#pragma unroll 1
  for(int i=0;i<SPIN_MAX;i++){ fence(); if(all_set(ld64(r,voff,off),tag)) break; }
  fence(); }
DI unsigned umax4(unsigned m,u32x4 v){ return max(max(max(max(m,v[0]),v[1]),v[2]),v[3]); }
template<class F,class G> DI void softmax_pm(const f32x16& p,u32x4(&o)[4],F side,G emit){
  auto mx3=[](float a,float b,float c){ return fmaxf(fmaxf(a,b),c); }; auto mn3=[](float a,float b,float c){ return fminf(fminf(a,b),c); };
  float mx=fmaxf(mx3(mx3(p[0],p[1],p[2]),mx3(p[3],p[4],p[5]),mx3(p[6],p[7],p[8])),mx3(mx3(p[9],p[10],p[11]),mx3(p[12],p[13],p[14]),p[15]));
  float mn=fminf(mn3(mn3(p[0],p[1],p[2]),mn3(p[3],p[4],p[5]),mn3(p[6],p[7],p[8])),mn3(mn3(p[9],p[10],p[11]),mn3(p[12],p[13],p[14]),p[15]));
  { auto a=__builtin_amdgcn_permlane32_swap(__float_as_uint(mx),__float_as_uint(mx),false,false), b=__builtin_amdgcn_permlane32_swap(__float_as_uint(mn),__float_as_uint(mn),false,false);
    mx=fmaxf(__uint_as_float(a[0]),__uint_as_float(a[1])); mn=fminf(__uint_as_float(b[0]),__uint_as_float(b[1])); }
  const float L=1.44269504088896f; const f32x2 Lp={L,L},Ln={-L,-L}; const float a=-mx*L,b=mn*L; const f32x2 a2={a,a},b2={b,b};
  f32x2 ep[8],en[8];
#pragma unroll
  for(int i=0;i<8;i++){ side(i); f32x2 x={p[2*i],p[2*i+1]}; ep[i]=fma2(x,Lp,a2); en[i]=fma2(x,Ln,b2);
    ep[i]=(f32x2){__builtin_amdgcn_exp2f(ep[i][0]),__builtin_amdgcn_exp2f(ep[i][1])}; en[i]=(f32x2){__builtin_amdgcn_exp2f(en[i][0]),__builtin_amdgcn_exp2f(en[i][1])};
    asm volatile("":"+v"(ep[i]),"+v"(en[i])::"memory"); }
  f32x2 sp=((ep[0]+ep[1])+(ep[2]+ep[3]))+((ep[4]+ep[5])+(ep[6]+ep[7])), sn=((en[0]+en[1])+(en[2]+en[3]))+((en[4]+en[5])+(en[6]+en[7]));
  const float rp=__builtin_amdgcn_rcpf(xh_add(sp[0]+sp[1])), rn=__builtin_amdgcn_rcpf(xh_add(sn[0]+sn[1]));
#pragma unroll
  for(int i=0;i<8;i++){ const f32x2* e=i<4?ep:en;
    o[i>>1][2*(i&1)]=pk2(e[2*(i&3)]*(i<4?rp:rn)); o[i>>1][2*(i&1)+1]=pk2(e[2*(i&3)+1]*(i<4?rp:rn)); emit(i); }
}
DI u32x4 pack8(const f32x16& x,int o){ u32x4 r={pk2(x[o],x[o+1]),pk2(x[o+2],x[o+3]),pk2(x[o+4],x[o+5]),pk2(x[o+6],x[o+7])}; return r; }
DI u32x4 pack44(const f32x16& a,int oa,const f32x16& b,int ob){ u32x4 r={pk2(a[oa],a[oa+1]),pk2(a[oa+2],a[oa+3]),pk2(b[ob],b[ob+1]),pk2(b[ob+2],b[ob+3])}; return r; }
DI f32x16 zero16(){ f32x16 z;
#pragma unroll
  for(int i=0;i<16;i++) z[i]=0.f;
  return z; }
DI f32x16 proj_phi(u32x4 a,u32x4 b,bf16x8 w0,bf16x8 w1){ u32x4 f0,f1;
#pragma unroll
  for(int i=0;i<4;i++){ auto s=__builtin_amdgcn_permlane16_swap(a[i],b[i],false,false); f0[i]=s[0]; f1[i]=s[1]; }
  return MFMA32(w1,asbf(f1),MFMA32(w0,asbf(f0),zero16())); }
DI f32x2 red8(const f32x2(&m)[4]){ f32x2 n[2]; float o[2];
#pragma unroll
  for(int j=0;j<4;j++){ auto t=__builtin_amdgcn_permlane32_swap(__float_as_uint(m[j>>1][j&1]),__float_as_uint(m[2+(j>>1)][j&1]),false,false); n[j>>1][j&1]=__uint_as_float(t[0])+__uint_as_float(t[1]); }
#pragma unroll
  for(int j=0;j<2;j++){ auto t=__builtin_amdgcn_permlane16_swap(__float_as_uint(n[0][j]),__float_as_uint(n[1][j]),false,false); o[j]=dpp_add<0x128>(__uint_as_float(t[0])+__uint_as_float(t[1])); }
  f32x2 r={o[0],o[1]}; return r; }
DI f32x2 stats64(const f32x2(&v)[8]){
  f32x2 s=((v[0]+v[1])+(v[2]+v[3]))+((v[4]+v[5])+(v[6]+v[7])); const float mu=xh_add(xg_add(s[0]+s[1]))*(1.f/64);
  f32x2 a={0,0},b={0,0};
#pragma unroll
  for(int i=0;i<8;i+=2){ f32x2 d=v[i]-mu, e=v[i+1]-mu; a=fma2(d,d,a); b=fma2(e,e,b); }
  a+=b; f32x2 r={mu,xh_add(xg_add(a[0]+a[1]))}; return r; }
DI void normalize(f32x2(&v)[8],const f32x2(&st)[4]){
  f32x2 s=(st[0]+st[1])+(st[2]+st[3]); const float mean=xh_add(xg_add(s[0]))*(1.f/16); float q=0;
#pragma unroll
  for(int i=0;i<4;i++){ float d=st[i][0]-mean; q=fmaf(d,d,q); }
#pragma unroll
  for(int i=0;i<8;i++) v[i]-=mean;
  const float rstd=__builtin_amdgcn_rsqf(xh_add(xg_add(fmaf(q,64.f,s[1])))*(1.f/1024)+1e-5f);
#pragma unroll
  for(int i=0;i<8;i++) v[i]*=rstd; }
#define LD_OUT [s]"+&s"(s),[so]"=&s"(so),[l]"=&s"(l),[t]"=&s"(t)
#define LD_IN [e]"s"(e),[rs]"s"(rs),[ts]"v"(ts),[tl]"v"(tl),[v0]"v"(v0)
DI void loop_a(int s,int e,u32x4 rs,unsigned ts,unsigned tl,unsigned v0){ unsigned so,l,t; asm volatile(LDA1:LD_OUT:LD_IN:"memory","scc"); }
template<int K> DI void loop_b(int s,int e,u32x4 rs,unsigned ts,unsigned tl,unsigned v0,unsigned v1,unsigned vh,unsigned d1,unsigned d2=0,unsigned d3=0){ unsigned so,l,t; v1-=1024;
  if(K==0) asm volatile(LDBQ:LD_OUT:LD_IN,[v1]"v"(v1),[sh]"s"(d1):"memory","scc");
  if(K==1) asm volatile(LDB1:LD_OUT:LD_IN,[v1]"v"(v1),[vh]"v"(vh),[sh]"s"(d1):"memory","scc");
  if(K==2) asm volatile(LDB2:LD_OUT:LD_IN,[v1]"v"(v1),[d1]"s"(d1),[d2]"s"(d2),[d3]"s"(d3):"memory","scc"); }
#define LANEVARS const int r=lane&15,g=lane>>4,q=r>>2,p=lane&3,hh=lane>>5,t32=lane&31,r8=(lane>>2)&7; const unsigned l16=lane*16, \
  chx=(((lane&3)^((r8>>2)<<1))+(hh<<2))*16  , blane=(g>>1)*1024+(4*(g&1)+q)*64+p*8  , \
  one=lane==0?0u:OOB

__global__ __launch_bounds__(512,2) void adaln_block(const u16* x,const u16* wqkv,const u16* wphi,const u16* wo,const u16* w1,const u16* b1,const u16* w2,const u16* b2,
                                                     const u16* cvec,const u16* wmod,u16* out,char* ws){
  const CAS u64* kp=(const CAS u64*)__builtin_amdgcn_kernarg_segment_ptr();
  u64 px,pc,pmod,pws,pc0; int bx=blockIdx.x,tid=threadIdx.x;
  asm volatile("s_load_dwordx2 %0, %6, 0x0\n\ts_load_dwordx2 %1, %6, 0x40\n\ts_load_dwordx2 %2, %6, 0x48\n\ts_load_dwordx2 %3, %6, 0x58\n\ts_getpc_b64 %4"
               :"=&s"(px),"=&s"(pc),"=&s"(pmod),"=&s"(pws),"=&s"(pc0),"+s"(bx):"s"(kp):"memory");
  asm volatile("":"+v"(tid));
  int lane=tid&63; const int wid=__builtin_amdgcn_readfirstlane(tid>>6)&7;
  const int xc=bx&7,bi=(bx>>3)&31;
  const int pm=8*(xc>>2)+(bi>>2), pn=4*(bi&3)+(xc&3);
  const int wr=(wid>>1)&1,wc=wid&1;
  const int hk=(pn+1)&15, hq=(pn+2)&15;
  u32x2 xr[4],tag; int utab;
  constexpr unsigned L_T2=16384;
  { LANEVARS;
  constexpr unsigned L_WM=16384, L_X=131072, L_S=147456, L_MT=148480, L_PHI=149504;
  unsigned cv; u64 ep;
  { unsigned vc=lane*2, vx0=(lane>>3)*2048+(((lane&7)^(lane>>4))<<4), vx1=(vx0^64)+8*2048-1024, vm=(8*(r8>>2)+(r8&3))*12288+chx;
    unsigned sc=pm*128, sx=(pm*128+wid*16)*2048+pn*128, lx=L_X+wid*2048, lm=L_WM+wid*1024,
             sm0=(pm*64+(wid>>2)*32+16*wc+4*wr)*12288, sm1=sm0+2048+((pn+2)&15)*128; sm0+=((pn+1)&15)*128;
    asm volatile("":"+v"(vc),"+v"(vx0),"+v"(vx1),"+v"(vm));
    asm volatile("s_waitcnt lgkmcnt(0)":"+s"(px),"+s"(pc),"+s"(pmod),"+s"(pws),"+s"(sc),"+s"(sx),"+s"(lx),"+s"(lm),"+s"(sm0),"+s"(sm1)::"memory");
    asm volatile("s_mov_b32 m0, %[lx]\n\ts_nop 0\n\tbuffer_load_dwordx4 %[vx0], %[rx], %[sx] offen lds\n\tbuffer_load_dwordx4 %[vx1], %[rx], %[sx] offen offset:1024 lds\n\t"
                 "s_mov_b32 m0, %[lm]\n\tbuffer_load_ushort %[cv], %[vc], %[rc], %[sc] offen\n\tbuffer_load_dwordx4 %[vm], %[rm], %[sm0] offen lds\n\t"
                 "s_add_i32 m0, %[lm], 8192\n\ts_nop 0\n\tbuffer_load_dwordx4 %[vm], %[rm], %[sm1] offen lds"
                 :[cv]"=&v"(cv),"+v"(lane)
                 :[lx]"s"(lx),[lm]"s"(lm),[vx0]"v"(vx0),[vx1]"v"(vx1),[vc]"v"(vc),[vm]"v"(vm),[sx]"s"(sx),[sc]"s"(sc),[sm0]"s"(sm0),[sm1]"s"(sm1),
                  [rx]"s"(mkrs(px,2048*2048)),[rc]"s"(mkrs(pc,2048)),[rm]"s"(mkrs(pmod,1024*12288)):"memory","scc");
    asm volatile("s_load_dwordx2 %0, %2, 0x0":"=&s"(ep),"+s"(kp):"s"(pws):"memory"); }
  const rsrc_t rw=mkr(pws,WS_SIZE); const u32x4 rws=mkrs(pws,WS_SIZE);
  { auto w=[&](int v){ return ((v+2*(pm&8))&16)|((v+2*pm)&15); };
    const int v0=((2*pn-2*(pm&8))&16)|((2*pn-2*pm)&15), l5=lane&31; utab=l5<2? 2*pn+l5 : w(l5-2<v0? l5-2 : l5); }
  auto unit1=[&](int s){ return __builtin_amdgcn_readlane(utab,s); };
  auto code_pf=[&](unsigned lds){ touch(mkrs(pc0,CLEN),lds,lane<16?(lane*32+bi)*128:OOB,0); };
  const int mt=(pn+3+(wid&3))&15; const unsigned modp_o=WS_MODP+(mt*16+pm)*1024+(wid&3)*256;
  const u32x4 rq=mkrs(kp[1],1024*6144);
  const unsigned vq0=(16*wc+r8)*6144+chx+hq*128, vq1=vq0+8*6144, lk=2048+(hk-hq)*128;
  auto issue_q=[&](int s){ unsigned so=unit1(s)*(32*6144), l=32768+(s&7)*12288+wc*2048;
#pragma unroll
    for(int L=0;L<3;L++){ unsigned o=so+(L?lk+(L-1)*2048:0); dma1(rq,l+L*4096,vq0,o); dma1(rq,l+L*4096+1024,vq1,o); } };
  auto parts_q=[&](int m){ if(m&&wid<6){
#pragma unroll
      for(int i=2;i<6;i++) if(m>>(i&3)&1){ unsigned so=unit1(i&3)*(32*6144)+(wid>=2?lk+(wid>>2)*2048:0), l=32768+(i&3)*12288+wc*2048+(wid>>1)*4096; dma1(rq,l,vq0,so); dma1(rq,l+1024,vq1,so); } } };
  static_assert((QM0|QM1|QM2)==15,"");
  asm volatile("s_waitcnt vmcnt(2) lgkmcnt(0)":"+s"(ep)::"memory"); asm volatile("":"+v"(cv));
#pragma unroll
  for(int n=0;n<4;n++) xr[n]=*(LAS u32x2*)(L_X+(wid*16+r)*128+(((2*n+(g>>1))^((r>>1)&7))<<4)+8*(g&1));
  parts_q(QM0);
  ep+=1; tag=(u32x2){(unsigned)ep,(unsigned)(ep>>32)};
  { float cf=blo(cv); float s=cf*__builtin_amdgcn_rcpf(1.f+__builtin_amdgcn_exp2f(-1.44269504088896f*cf)); *(LAS u16*)(L_S+wid*128+lane*2)=(u16)pk2(s,s); }
  const bf16x8 sf0=lds128(L_S+wid*128+g*16), sf1=lds128(L_S+wid*128+64+g*16);
  f32x2 xv[8];
#pragma unroll
  for(int i=0;i<8;i++) xv[i]=up2(xr[i>>1][i&1]);
  { f32x2 st=stats64(xv); u32x4 o={tag[0],__float_as_uint(st[0]),tag[0],__float_as_uint(st[1])};
    st128(rw,o,lane<16?l16:OOB,WS_G2+(pm*16+pn)*2048+wid*256); }
  { unsigned a=L_WM+(wid>>2)*8192+wr*512+blane+(((wid^g)&1)*32), vo=r<2?(g*2+r)*16:OOB, so=WS_G1+(((pn+1+(wid>>2))&15)*16+pm)*1024+wid*128;
    asm volatile("":"+v"(a),"+v"(vo)); asm volatile("":"+s"(so));
    if(wid<6) waitvm<1+2*__builtin_popcount(QM0)>(); else waitvm<1>();
    bar();
    f32x4 c={0,0,0,0}; c=MFMA16(ldstr(a,a+2048),sf0,c); c=MFMA16(ldstr(a+4096,a+6144),sf1,c);
    u32x4 v={tag[0],__float_as_uint(r?c[2]:c[0]),tag[0],__float_as_uint(r?c[3]:c[1])};
    st128(rw,v,vo,so); }
  if(wid>=6){ const u32x4 rp=mkrs(kp[2],65536);
#pragma unroll
    for(int i=0;i<4;i++) dma1(rp,L_PHI+wc*4096+i*1024,l16,(wc?hq:hk)*4096+i*1024);
    if(wc) code_pf(158720); }
  parts_q(QM1);
  __builtin_amdgcn_s_sleep(SLP1);
  { u32x4 a,b,c[4];
#pragma unroll 1
    for(int it=0;it<SPIN_MAX;it++){ fence();
      a=ld128(rw,(lane>>3)*2048+(lane&7)*16,WS_G1+pn*16384+wid*128); b=ld128(rw,(lane>>3)*2048+(lane&7)*16+1024,WS_G1+pn*16384+wid*128);
#pragma unroll
      for(int i=0;i<4;i++) c[i]=ld128(rw,g*8192+r*16,WS_G2+pm*32768+i*2048+wid*256);
      unsigned d=(a[0]^tag[0])|(a[2]^tag[0])|(b[0]^tag[0])|(b[2]^tag[0]);
#pragma unroll
      for(int i=0;i<4;i++) d|=(c[i][0]^tag[0])|(c[i][2]^tag[0]);
      if(none(d!=0)) break; }
    { auto t=__builtin_amdgcn_permlane32_swap(__float_as_uint(__uint_as_float(a[1])+__uint_as_float(b[1])),__float_as_uint(__uint_as_float(a[3])+__uint_as_float(b[3])),false,false);
      float m=rbf(xg_add(dpp_add<0x128>(__uint_as_float(t[0])+__uint_as_float(t[1])))); if(wid>=4) m=rbf(1.f+m);
      *(LAS float*)(L_MT+wid*64+(lane&7)*8+hh*4)=m; }
    f32x2 st[4];
#pragma unroll
    for(int i=0;i<4;i++) st[i]=(f32x2){__uint_as_float(c[i][1]),__uint_as_float(c[i][3])};
    normalize(xv,st);
    unsigned a0=L_MT+g*16, so=WS_H+((pm*32+2*pn)*8+wid)*1024, fo=WS_FH+pm*256+pn*8, vf=wid==4?one:OOB;
    asm volatile("":"+v"(xv[0]),"+v"(xv[1]),"+v"(xv[2]),"+v"(xv[3]),"+v"(xv[4]),"+v"(xv[5]),"+v"(xv[6]),"+v"(xv[7]),"+v"(a0),"+v"(vf)); asm volatile("":"+s"(so),"+s"(fo));
    lbar();
    u32x4 o[2];
#pragma unroll
    for(int n=0;n<4;n++){ f32x4 sh=*(LAS f32x4*)(a0+n*64), sc=*(LAS f32x4*)(a0+256+n*64);
      o[n>>1][2*(n&1)]=pk2(fma2(xv[2*n],half2(sc,0),half2(sh,0))); o[n>>1][2*(n&1)+1]=pk2(fma2(xv[2*n+1],half2(sc,1),half2(sh,1)));
      if(n&1) st128(rw,o[n>>1],l16,so+(n>>1)*8192); }
    *(LAS u32x4*)(wid*1024+l16)=o[0]; *(LAS u32x4*)(8192+wid*1024+l16)=o[1];
    if(QM2){ bar(); parts_q(QM2); if(wid<6) waitvm<2*__builtin_popcount(QM2)>(); else waitvm<0>(); } else waitvm<0>();
    lbar();
    if(wid==4) st64(rw,tag,vf,fo); }
  asm volatile("":"+v"(xr[0]),"+v"(xr[1]),"+v"(xr[2]),"+v"(xr[3]));
  { const u32x4 ones={~0u,~0u,~0u,~0u};
    st128(rw,ones,l16,WS_KVP+((hk*8+wid)*16+pm)*1024);
    unsigned o1=WS_KZ+((hk*8+wid)*16+pm)*64, o2=modp_o+(wid>>2)*128-64, o3=WS_ST2+(pm*16+pn)*1024+wid*128-192, o5=WS_KVS+hk*8192+pm*512+wid*64-320;
    asm volatile("":"+s"(o1),"+s"(o2),"+s"(o3),"+s"(o5));
    unsigned o4=lane<24?o5:OOB; o4=lane<20?o3:o4; o4=lane<12?o2:o4; o4=lane<4?o1:o4;
    st128(rw,ones,o4+l16,0); }
  bf16x8 wf0,wf1;
  { unsigned a=L_PHI+(wid>>2)*4096+wc*2048+(8*hh+q)*64+(g&1)*32+p*8; wf0=ldstr(a,a+1024); wf1=ldstr(a+256,a+1280); }
  constexpr unsigned L_PK=0, L_Q=131072, L_V=147456, L_ZF=8192;
  const u32x4 ro=mkrs(kp[3],1024*2048);
  const unsigned vo=(16*(r8>>2)+(r8&3))*2048+chx+pn*128;
  auto issue_o=[&](int j,int RG){ dma1(ro,32768+j*4096+RG*1024,vo,(((j+2*pm)&31)*32+8*(RG>>1)+4*(RG&1))*2048); };
  unsigned kva,kvv=L_V+wc*8192+l16, kvo=((g>>1)+2*(g&1))*256+r*16, kzo=r==0?g*16:OOB;
  { const int tau=4*g+q; kva=L_PK+(wc*128+tau)*128+(((4*(wid>>1)+p)^((((tau>>1)&3)<<2)|(tau&1)|(((tau>>3)&1)<<1)))<<3); }
  asm volatile("":"+v"(kva),"+v"(kvv),"+v"(kvo),"+v"(kzo));
  auto kv_stage=[&](){
    f32x4 k0={0,0,0,0},k1=k0,kz=k0; const u32x4 unit={0x3f803f80u,0x3f803f80u,0x3f803f80u,0x3f803f80u};
    bf16x8 f[4],v0[4],v1[4];
#pragma unroll
    for(int t=0;t<4;t++){ f[t]=ldstr(kva+t*4096,kva+t*4096+2048); v0[t]=lds128(kvv+t*2048); v1[t]=lds128(kvv+t*2048+1024); }
#pragma unroll
    for(int t=0;t<4;t++){ k0=MFMA16(f[t],v0[t],k0); k1=MFMA16(f[t],v1[t],k1); }
#pragma unroll
    for(int t=0;t<4;t++) kz=MFMA16(f[t],asbf(unit),kz);
    auto s0=__builtin_amdgcn_permlane32_swap(pk2(k0[0],k0[1]),pk2(k1[0],k1[1]),false,false), s1=__builtin_amdgcn_permlane32_swap(pk2(k0[2],k0[3]),pk2(k1[2],k1[3]),false,false);
    u32x4 o={s0[0],s1[0],s0[1],s1[1]};
    st128(rw,o,kvo,WS_KVP+((hk*8+wid)*16+pm)*1024);
    st128(rw,__builtin_bit_cast(u32x4,kz),kzo,WS_KZ+((hk*8+wid)*16+pm)*64); };
  if(wid<4){
    unsigned a0=wr*4096+l16, b0e0=32768+wc*512+blane+((g&1)*32), b0e1=b0e0^32, b1e0=b0e0+4*12288, b1e1=b0e1+4*12288;
    f32x16 c0,c1,c2,c3,c4,c5; int cnt;
    unsigned va=L_V+(wc*4+wr*2)*2048+l16, qa=L_Q+wid*4096+l16, pa[8];
#pragma unroll
    for(int i=0;i<8;i++) pa[i]=(L_PK+(wc*128+wr*64+t32)*128+((((((t32>>1)&3)<<2)|(t32&1)|(((t32>>3)&1)<<1))^hh)<<3))^(i*16);
    asm volatile("":"+v"(va),"+v"(qa),"+v"(pa[0]),"+v"(pa[1]),"+v"(pa[2]),"+v"(pa[3]),"+v"(pa[4]),"+v"(pa[5]),"+v"(pa[6]),"+v"(pa[7]));
    waitvm<0>();
    asm volatile(QKV_ASM : "=&{v[0:15]}"(c0),"=&{v[16:31]}"(c1),"=&{v[32:47]}"(c2),"=&{v[48:63]}"(c3),"=&{v[64:79]}"(c4),"=&{v[80:95]}"(c5),[cnt]"=&s"(cnt)
                 : [a0]"v"(a0),[b0e0]"v"(b0e0),[b0e1]"v"(b0e1),[b1e0]"v"(b1e0),[b1e1]"v"(b1e1) : "memory","scc",QKV_CLOB);
    { f32x16 pk0=proj_phi(pack44(c2,0,c3,0),pack44(c2,4,c3,4),wf0,wf1); asm volatile("":"+v"(pk0));
      f32x16 pk1=proj_phi(pack44(c2,8,c3,8),pack44(c2,12,c3,12),wf0,wf1); asm volatile("":"+v"(pk1));
      u32x4 o0[4],o1[4];
      auto put=[&](const u32x4(&o)[4],int i,int t){ u32x2 v={o[i>>1][2*(i&1)],o[i>>1][2*(i&1)+1]}; *(LAS u32x2*)(pa[i]+t*4096)=v; };
      softmax_pm(pk0,o0,[&](int i){ if(!(i&1)) *(LAS u32x4*)(va+i*512)=pack8((i&2)?c5:c4,8*(i>>2)); },[](int){});
      softmax_pm(pk1,o1,[&](int i){ put(o0,i,0); },[&](int i){ put(o1,i,1); __builtin_amdgcn_sched_barrier(0); }); }
    lbar();
    kv_stage();
#pragma unroll
    for(int m=0;m<4;m++) *(LAS u32x4*)(qa+m*1024)=pack44(c0,4*m,c1,4*m);
    lbar();
    if(bx==0&&wid==0) st64(rw,tag,one,WS_E);
    u32x4 za,zb,a,b;
    unsigned vs=(lane>>3)*2048+(lane&7)*16, ss=WS_KVP+(hk*8+(pm>>1))*16384+(pm&1)*512+wid*128, vk=(lane&8)?OOB:(lane&7)*16+g*4, sk=WS_KVS+hk*8192+pm*512+wid*128;
    asm volatile("":"+v"(vs),"+v"(vk)); asm volatile("":"+s"(ss),"+s"(sk));
    auto ldz=[&](){ za=ld128(rw,r*64+g*16,WS_KZ+(hq*8+2*wid)*1024); zb=ld128(rw,r*64+g*16+1024,WS_KZ+(hq*8+2*wid)*1024); };
    __builtin_amdgcn_s_sleep(SLP2);
#pragma unroll 1
    for(int it=0;it<SPIN_MAX;it++){ fence();
      a=ld128(rw,vs,ss); b=ld128(rw,vs+1024,ss); ldz();
      if(none(umax4(umax4(0,a),b)==~0u)) break; }
    { f32x2 m[4];
#pragma unroll
      for(int j=0;j<4;j++) m[j]=up2(a[j])+up2(b[j]);
      __builtin_amdgcn_raw_buffer_store_b32(pk2(red8(m)),rw,vk,sk,16); }
#pragma unroll 1
    for(int it=0;it<SPIN_MAX;it++){ if(none(umax4(umax4(0,za),zb)==~0u)) break; fence(); ldz(); }
#pragma unroll
    for(int e=0;e<2;e++){ u32x4 zz=e?zb:za;
      u32x2 oz={pk2(row_sum(__uint_as_float(zz[0])),row_sum(__uint_as_float(zz[1]))),pk2(row_sum(__uint_as_float(zz[2])),row_sum(__uint_as_float(zz[3])))};
      *(LAS u32x2*)(L_ZF+(e*4+wid)*32+(g&1)*16+8*(g>>1))=oz; }
    lbar();
#pragma unroll 1
    for(int j=24;j<32;j++) issue_o(j,wid);
    bar();
    unsigned mp[16];
    auto ldp=[&](){
#pragma unroll
      for(int i=0;i<16;i++) mp[i]=__builtin_amdgcn_raw_buffer_load_b32(rw,lane*4+(i&3)*1024,WS_MODP+pn*16384+(i>>2)*4096+wid*256,16); };
    ldp();
    bar();
#pragma unroll 1
    for(int it=0;it<SPIN_MAX;it++){ unsigned mx=0;
#pragma unroll
      for(int i=0;i<16;i++) mx=max(mx,mp[i]);
      if(none(mx==~0u)) break;
      fence(); ldp(); }
    float m=0;
#pragma unroll
    for(int i=0;i<16;i+=4) m+=(__uint_as_float(mp[i])+__uint_as_float(mp[i+1]))+(__uint_as_float(mp[i+2])+__uint_as_float(mp[i+3]));
    m=rbf(m); if(wid==2) m=rbf(1.f+m);
    *(LAS float*)(L_T2+wid*256+lane*4)=m;
  } else {
    float sk[8];
#pragma unroll
    for(int i=0;i<8;i++) sk[i]=blo(*(LAS u16*)(L_S+wid*128+i*16+(lane>>3)*2));
    if(wid<6){
      const unsigned va=l16+wc*4096, ts=WS_H+(pm*32+utab)*8192, tl=(lane&3)*8192+wc*4096;
      unsigned s2=WS_H+(pm*32+unit1(2))*8192, s4=WS_H+(pm*32+unit1(4))*8192, fo=WS_FH+pm*256+(unit1(2)>>1)*8;
      asm volatile("":"+s"(s2),"+s"(s4),"+s"(fo));
      wait_flags(rw,fo,0,tag);
      dma4(rws,16384+wc*4096,va,s2); dma4(rws,24576+wc*4096,va,s2+8192);
      bar(); bar();
      wait_flags(rw,WS_FH+pm*256,r*8,tag);
      dma4(rws,wc*4096,va,s4);
      if(wid==4) st64(rw,tag,one,WS_FC+pm*256+pn*8);
      loop_a(5,32,rws,ts,tl,va);
      bar(); code_pf(0); waitvm<5>(); bar(); waitvm<0>(); bar(); bar();
    } else {
#pragma unroll 1
      for(int s=4;s<8;s++) issue_q(s);
      waitvm<24>();
      bar();
      loop_b<0>(8,32,rq,utab*(32*6144),32768+(lane&7)*12288+wc*2048,vq0,vq1,0,lk);
#pragma unroll 1
      for(int s=0;s<8;s++){ bar();
#pragma unroll
        for(int j=0;j<3;j++){ issue_o(3*s+j,2*wc); issue_o(3*s+j,2*wc+1); }
        if(s<6) waitvm<36>(); }
    }
    wait_flags(rw,WS_FC+(wid&3)*1024,g*256+r*8,tag);
    u32x4 mw[8];
    { const rsrc_t rmod=mkr(pmod,1024*12288);
#pragma unroll
      for(int i=0;i<8;i++) mw[i]=__builtin_amdgcn_raw_buffer_load_b128(rmod,(lane>>3)*12288+(lane&7)*16,(pm*64+i*8)*12288+(2+(wid&3))*2048+mt*128,0); }
    bar();
    kv_stage();
    lbar();
    u32x4 qf[4],ph[2][4];
#pragma unroll
    for(int m=0;m<4;m++) qf[m]=*(LAS u32x4*)(L_Q+(wid&3)*4096+m*1024+l16);
    { f32x16 pq0=proj_phi(qf[0],qf[1],wf0,wf1); asm volatile("":"+v"(pq0));
      f32x16 pq1=proj_phi(qf[2],qf[3],wf0,wf1); asm volatile("":"+v"(pq1));
#pragma unroll
      for(int t=0;t<2;t++){ softmax_pm(t?pq1:pq0,ph[t],[](int){},[](int){});
        asm volatile("":"+v"(ph[t][0]),"+v"(ph[t][1]),"+v"(ph[t][2]),"+v"(ph[t][3])); } }
    { f32x2 m[4];
#pragma unroll
      for(int j=0;j<4;j++){ m[j]=up2(mw[0][j])*sk[0];
#pragma unroll
        for(int i=1;i<8;i++) m[j]=fma2(up2(mw[i][j]),(f32x2){sk[i],sk[i]},m[j]); }
      st64(rw,__builtin_bit_cast(u32x2,red8(m)),(lane&8)?OOB:(lane&7)*32+g*8,modp_o); }
    u32x4 kf[4];
    auto ldk=[&](){ fence();
#pragma unroll
      for(int s=0;s<4;s++) kf[s]=ld128(rw,l16,WS_KVS+hq*8192+(2*s+wc)*1024);
      fence(); };
    __builtin_amdgcn_s_sleep(SLP6);
    ldk();
    bar();
    float inv[2]; unsigned vo2=(g&1)*1024+hh*512+r*16, so=WS_O+((pm*32+2*hq+wc)*8+wr*4)*1024, fo=WS_FO+pm*256+pn*8, vf=one;
    { f32x16 d0=zero16(),d1=d0;
#pragma unroll
      for(int s=0;s<4;s++){ bf16x8 z=lds128(L_ZF+(wc*4+s)*32+hh*16); d0=MFMA32(z,asbf(ph[0][s]),d0); d1=MFMA32(z,asbf(ph[1][s]),d1); }
      inv[0]=__builtin_amdgcn_rcpf(d0[0]+1e-6f); inv[1]=__builtin_amdgcn_rcpf(d1[0]+1e-6f);
      asm volatile("":"+v"(inv[0]),"+v"(inv[1]),"+v"(vo2),"+v"(vf)); asm volatile("":"+s"(so),"+s"(fo)); }
#pragma unroll 1
    for(int it=0;it<SPIN_MAX;it++){ if(none(umax4(umax4(umax4(umax4(0,kf[0]),kf[1]),kf[2]),kf[3])==~0u)) break; ldk(); }
    { f32x16 n[2];
#pragma unroll
      for(int t=0;t<2;t++){ n[t]=zero16();
#pragma unroll
        for(int s=0;s<4;s++) n[t]=MFMA32(asbf(kf[s]),asbf(ph[t][s]),n[t]);
        asm volatile("":"+v"(n[t])); }
#pragma unroll
      for(int t=0;t<2;t++){ n[t]*=inv[t]; st128(rw,pack8(n[t],0),vo2+t*2048,so); st128(rw,pack8(n[t],8),vo2+t*2048+256,so); } }
    bar();
    waitvm<0>(); bar();
    if(wid==4) st64(rw,tag,vf,fo);
  }
  lbar();
  }
  asm volatile("":"+v"(lane)); asm volatile("":"+s"(kp),"+s"(pws));
  LANEVARS;
  const rsrc_t rw=mkr(pws,WS_SIZE); const u32x4 rws=mkrs(pws,WS_SIZE), r1=mkrs(kp[4],1024*7168);
  auto unit1=[&](int s){ return __builtin_amdgcn_readlane(utab,s); };
  auto code_pf=[&](unsigned lds){ touch(mkrs(pc0,CLEN),lds,lane<16?(lane*32+bi)*128:OOB,0); };
  constexpr unsigned L_X1=147456;
  const unsigned v10=(16*wc+r8)*7168+chx+pn*448+(pn&1)*64, v11=v10+8*7168,
                 v1h=(16*wc+(lane>>2))*7168+(((lane&3)^((r8>>2)<<1))*16)+pn*448+((pn&1)?0:384);
  f32x4 gb0=*(LAS f32x4*)(L_T2+768+wc*128+g*16), gb1=*(LAS f32x4*)(L_T2+768+wc*128+64+g*16);
  u32x2 bv[2];
  { f32x4 tga[4],tsb[4],tg2[4]; f32x2 xv[8];
#pragma unroll
    for(int n=0;n<4;n++){ tga[n]=*(LAS f32x4*)(L_T2+n*64+g*16); tsb[n]=*(LAS f32x4*)(L_T2+256+n*64+g*16); tg2[n]=*(LAS f32x4*)(L_T2+512+n*64+g*16); }
#pragma unroll
    for(int n=0;n<2;n++) bv[n]=__builtin_amdgcn_raw_buffer_load_b64(mkr(kp[7],2048),wid>=4?g*8:OOB,pn*128+wc*64+n*32,0);
#pragma unroll
    for(int i=0;i<8;i++) xv[i]=up2(xr[i>>1][i&1]);
    f32x16 c,co; int cnt,tt;
    unsigned e0=32768+wc*65536+blane+((g&1)*32), e1=e0^32, rot=((2*pm+16*wc)&31)*8192, so=WS_O+(pm*256+wid)*1024, vl=l16, w0=wid<6?v10:v1h, w1_=wid<6?v11:v1h,
             l0=133120+(wid<6? (wid>>1)*4096+wc*2048 : 12288+wc*1024), l1=wid<6?l0+1024:l0, lo=wid<6? (wid>>1)*128 : 0, s0=unit1(2)*(32*7168)+lo, s2=2*pn*(32*7168)+lo,
             xo=(wid^1)*4096+l16, xi=wid*4096+l16, vs=lane<16?lane*8:OOB;
    asm volatile("":"+v"(e0),"+v"(e1),"+v"(vl),"+v"(w0),"+v"(w1_),"+v"(xo),"+v"(xi),"+v"(vs),"+v"(xv[0]),"+v"(xv[1]),"+v"(xv[2]),"+v"(xv[3]),"+v"(xv[4]),"+v"(xv[5]),"+v"(xv[6]),"+v"(xv[7]),"+v"(bv[0]),"+v"(bv[1])); asm volatile("":"+s"(rot),"+s"(so),"+s"(l0),"+s"(l1),"+s"(s0),"+s"(s2));
    if(wid==4) wait_flags(rw,WS_FO+pm*256,r*8,tag);
    lbar();
    asm volatile(WO_ASM : "=&{v[0:15]}"(c),"=&{v[16:31]}"(co),[e0]"+v"(e0),[e1]"+v"(e1),[rot]"+s"(rot),[cnt]"=&s"(cnt),[t]"=&s"(tt)
                 : [vo]"v"(vl),[rs]"s"(rws),[so]"s"(so),[rw]"s"(r1),[w0]"v"(w0),[w1]"v"(w1_),[l0]"s"(l0),[l1]"s"(l1),[s0]"s"(s0),[s1]"s"(s0+32*7168) : "memory","scc",WO_CLOB);
#pragma unroll
    for(int n=0;n<4;n++){ f32x4 t={co[4*n],co[4*n+1],co[4*n+2],co[4*n+3]}; *(LAS f32x4*)(xo+n*1024)=t; }
    lbar();
    u32x4 x1p[2];
#pragma unroll
    for(int n=0;n<4;n++){ f32x4 t=*(LAS f32x4*)(xi+n*1024);
#pragma unroll
      for(int i=0;i<2;i++){ f32x2 a={c[4*n+2*i],c[4*n+2*i+1]}; unsigned u=pk2(fma2(half2(tga[n],i),a+half2(t,i),xv[2*n+i])); x1p[n>>1][2*(n&1)+i]=u; xv[2*n+i]=up2(u); } }
    st64(rw,__builtin_bit_cast(u32x2,stats64(xv)),vs,WS_ST2+(pm*16+pn)*1024+wid*128);
    bar();
    dma1(r1,l0-6*14336,w0,s2); dma1(r1,l0-7*14336,w0,s2+32*7168);
    if(wid<6){ dma1(r1,l1-6*14336,w1_,s2); dma1(r1,l1-7*14336,w1_,s2+32*7168); }
    *(LAS u32x4*)(L_X1+wid*1024+l16)=x1p[0]; *(LAS u32x4*)(L_X1+8192+wid*1024+l16)=x1p[1];
    unsigned sh=WS_H2+((pm*32+2*pn)*8+wid)*1024, fo=WS_FH2+pm*256+pn*8, vf=one;
    asm volatile("":"+v"(vf)); asm volatile("":"+s"(sh),"+s"(fo));
    u32x2 sv[4];
#pragma unroll 1
    for(int it=0;it<SPIN_MAX;it++){ fence(); unsigned m=0;
#pragma unroll
      for(int i=0;i<4;i++){ sv[i]=ld64(rw,g*4096+r*8+i*1024,WS_ST2+pm*16384+wid*128); m=max(max(m,sv[i][0]),sv[i][1]); }
      if(none(m==~0u)) break; }
    f32x2 st[4];
#pragma unroll
    for(int i=0;i<4;i++) st[i]=__builtin_bit_cast(f32x2,sv[i]);
    normalize(xv,st);
    u32x4 o[2];
#pragma unroll
    for(int n=0;n<4;n++){
      o[n>>1][2*(n&1)]=pk2(fma2(xv[2*n],half2(tg2[n],0),half2(tsb[n],0))); o[n>>1][2*(n&1)+1]=pk2(fma2(xv[2*n+1],half2(tg2[n],1),half2(tsb[n],1)));
      if(n&1) st128(rw,o[n>>1],l16,sh+(n>>1)*8192); }
    *(LAS u32x4*)(wid*1024+l16)=o[0]; *(LAS u32x4*)(8192+wid*1024+l16)=o[1];
    waitvm<0>(); lbar();
    if(wid==4) st64(rw,tag,vf,fo); }
  const int kq=pn&3;
  constexpr unsigned L_A2=32768, L_OWN=131072;
  if(wid<4){
    auto peer=[&](int L){ return pm*16+(pn&12)+((kq+1+L)&3); };
    unsigned a2=L_A2+wr*4096+l16, e20=98304+wc*512+blane+((g&1)*32), e21=e20^32, vx=l16, vf=one; u32x2 tv=tag;
    unsigned x0=WS_XP+(peer(0)*12+wid)*4096, x1=WS_XP+(peer(1)*12+4+wid)*4096, x2=WS_XP+(peer(2)*12+8+wid)*4096,
             xf0=WS_FX+peer(0)*512+wid*128, xf1=WS_FX+peer(1)*512+wid*128+8, xf2=WS_FX+peer(2)*512+wid*128+16;
    asm volatile("":"+v"(a2),"+v"(e20),"+v"(e21),"+v"(vx),"+v"(vf),"+v"(tv)); asm volatile("":"+s"(x0),"+s"(x1),"+s"(x2),"+s"(xf0),"+s"(xf1),"+s"(xf2));
    { unsigned a0=wr*4096+l16, b0e0=32768+wc*512+blane+((g&1)*32), b0e1=b0e0^32, b1e0=b0e0+4*14336, b1e1=b0e1+4*14336;
      unsigned h0=32768+(g>>1)*512+(4*(g&1)+q)*64+p*8+(((wc^g)&1)*32), h1=h0+4*14336;
      unsigned lb=L_A2+wr*4096+l16+wc*8192, lh=L_A2+wr*4096+l16+wc*8+2*8192; float inf=__builtin_inff(); asm volatile("":"+v"(lb),"+v"(lh)); asm volatile("":"+s"(inf));
      f32x16 c[7]; int cnt;
      const rsrc_t rb=mkr(kp[5],7168);
#pragma unroll
      for(int n=0;n<7;n++){ unsigned co=n<6? pn*448+(pn&1)*64+(n>>1)*128+wc*64+(n&1)*32 : pn*448+((pn&1)?0:384)+wc*32;
        u32x2 b=__builtin_amdgcn_raw_buffer_load_b64(rb,g*8,co,0);
#pragma unroll
        for(int m=0;m<4;m++){ c[n][4*m]=blo(b[0]); c[n][4*m+1]=bhi(b[0]); c[n][4*m+2]=blo(b[1]); c[n][4*m+3]=bhi(b[1]); } }
      asm volatile(FF1_ASM : "+{v[0:15]}"(c[0]),"+{v[16:31]}"(c[1]),"+{v[32:47]}"(c[2]),"+{v[48:63]}"(c[3]),"+{v[64:79]}"(c[4]),"+{v[80:95]}"(c[5]),"+{v[96:111]}"(c[6]),[cnt]"=&s"(cnt)
                   : [a0]"v"(a0),[b0e0]"v"(b0e0),[b0e1]"v"(b0e1),[b1e0]"v"(b1e0),[b1e1]"v"(b1e1),[h0]"v"(h0),[h1]"v"(h1) : "memory","scc",FF1_CLOB);
      auto act=[&](int n,int i){ f32x2 v={__builtin_amdgcn_fmed3f(c[n][i],0.f,inf),__builtin_amdgcn_fmed3f(c[n][i+1],0.f,inf)}; return pk2(v*v); };
#pragma unroll
      for(int L=0;L<3;L++)
#pragma unroll
        for(int m=0;m<4;m++){ u32x4 o={act(2*L,4*m),act(2*L,4*m+2),act(2*L+1,4*m),act(2*L+1,4*m+2)};
          asm volatile("":"+v"(o)::"memory");
          if(m==0&&L==1) lbar();
          *(LAS u32x4*)(lb+((L+2)&3)*16384+m*1024)=o;
          __builtin_amdgcn_sched_barrier(0); }
#pragma unroll
      for(int m=0;m<4;m++){ u32x2 v={act(6,4*m),act(6,4*m+2)}; *(LAS u32x2*)(lh+m*1024)=v; __builtin_amdgcn_sched_barrier(0); }
    }
    f32x16 c[2]; int cnt;
    asm volatile("s_waitcnt lgkmcnt(0)\n\t" FF2_ASM : "=&{v[96:111]}"(c[0]),"=&{v[112:127]}"(c[1]),[cnt]"=&s"(cnt)
                 : [a0]"v"(a2),[b0e0]"v"(e20),[b0e1]"v"(e21),[vx]"v"(vx),[rs]"s"(rws),[x0]"s"(x0),[x1]"s"(x1),[x2]"s"(x2),[tg]"v"(tv),[vf]"v"(vf),[f0]"s"(xf0),[f1]"s"(xf1) : "memory","scc",FF2_CLOB);
    waitvm<1>(); st64(rw,tv,vf,xf2);
    unsigned lo=L_OWN+wid*8192+vx; asm("":"+v"(lo));
#pragma unroll
    for(int i=0;i<8;i++){ f32x4 v={c[i>>2][4*(i&3)],c[i>>2][4*(i&3)+1],c[i>>2][4*(i&3)+2],c[i>>2][4*(i&3)+3]}; *(LAS f32x4*)(lo+i*1024)=v; }
    lbar();
    return;
  }
  f32x2 c[16];
  auto gate=[&](int k){ return half2((k&2)?gb1:gb0,k&1); };
  auto base=[&](){
#pragma unroll
    for(int m=0;m<4;m++){ u32x4 xv=*(LAS u32x4*)(L_X1+wc*8192+(wr*4+m)*1024+l16);
#pragma unroll
      for(int k=0;k<4;k++) c[4*m+k]=fma2(gate(k),up2(bv[k>>1][k&1]),up2(xv[k]));
      asm volatile("":"+v"(c[4*m]),"+v"(c[4*m+1]),"+v"(c[4*m+2]),"+v"(c[4*m+3])::"memory"); } };
  int spr,suu;
  { int j=t32-7, d=(j*43)>>7, k=j-3*d+pm%3; k=k>=3?k-3:k; suu=t32<7?t32:d; spr=t32<7?pn:((pn+4+4*k)&15); }
  const u32x4 r2=mkrs(kp[6],3584*2048);
  if(wid<6){
    const unsigned va=l16+wc*4096;
    { const unsigned ts=WS_H2+(pm*32+utab)*8192, tl=(lane&3)*8192+wc*4096;
      unsigned s2=WS_H2+(pm*32+unit1(2))*8192, s4=WS_H2+(pm*32+unit1(4))*8192, fo=WS_FH2+pm*256+(unit1(2)>>1)*8;
      asm volatile("":"+s"(s2),"+s"(s4),"+s"(fo));
      base();
      wait_flags(rw,fo,0,tag);
      dma4(rws,16384+wc*4096,va,s2); dma4(rws,24576+wc*4096,va,s2+8192);
      bar(); bar();
      wait_flags(rw,WS_FH2+pm*256,r*8,tag);
      dma4(rws,wc*4096,va,s4);
      loop_a(5,32,rws,ts,tl,va);
      auto warm=[&](int k){ unsigned o=(k*64+lane)*128; touch(r2,0,o<28672?o:OOB,bx*28672); };
      bar(); warm(wc); code_pf(0); waitvm<6>(); bar(); warm(2+wc); waitvm<3>(); bar(); bar(); }
    const int ts=pm*112+spr*7+suu; const unsigned h1o=WS_H1+(pm*112+pn*7)*8192, f1o=WS_FH1+pm*256+kq*64;
    auto issue=[&](int s){ dma4(rws,L_A2+((s+4)&7)*8192+wc*4096,va,__builtin_amdgcn_readlane(ts,s)*8192+WS_H1); };
    auto copy=[&](int k){ u32x4 v[2]; const int u=k>>1;
#pragma unroll
      for(int i=0;i<2;i++) v[i]=*(LAS u32x4*)(L_A2+((u+4)&7)*8192+((k&1)*2+i)*1024+va);
#pragma unroll
      for(int i=0;i<2;i++) st128(rw,v[i],va+((k&1)*2+i)*1024,h1o+u*8192); };
    auto raise=[&](int pc){ st64(rw,tag,one,f1o+pc*4096+(pn>>2)*16+wc*8); };
    u32x2 f;
    auto poll=[&](int pc){ asm volatile("buffer_load_dwordx2 %0, %1, %2, %3 offen sc1":"=v"(f):"v"((lane&7)*8),"s"(rws),"s"(f1o+pc*4096):"memory"); };
    auto check=[&](int pc){ asm volatile("":"+v"(f)::"memory"); if(!all_set(f,tag)) wait_flags(rw,f1o+pc*4096,(lane&7)*8,tag); };
    bar();
    copy(0); copy(1); copy(2); copy(3);
    bar();
    bar(); copy(4);
    bar(); waitvm<2>(); raise(0); copy(5);
    bar(); copy(6);
    bar(); poll(0); copy(7);
    bar(); waitvm<2>(); check(0); issue(7); issue(8); copy(8);
    bar(); issue(9); copy(9); waitvm<12>(); raise(1);
    bar(); issue(10); copy(10); waitvm<15>();
    bar(); issue(11); copy(11); waitvm<15>();
    bar(); poll(1); issue(12); copy(12); waitvm<15>();
    bar(); waitvm<6>(); check(1); issue(13); copy(13);
    bar(); issue(14); waitvm<12>();
    bar(); issue(15); waitvm<10>();
    bar(); issue(16); waitvm<8>(); raise(2);
    bar(); issue(17); code_pf(0); waitvm<9>();
    bar(); poll(2); issue(18); waitvm<9>();
    bar(); waitvm<4>(); check(2); issue(19); issue(20);
    bar(); issue(21); issue(22); waitvm<16>();
    bar(); issue(23); issue(24); waitvm<20>();
    bar(); issue(25); issue(26); waitvm<16>();
    bar(); issue(27); waitvm<4>();
    bar(); waitvm<0>();
    bar(); bar(); bar(); bar();
  } else {
    const unsigned v20=(16*wc+r8)*2048+chx+(pn>>2)*512, v21=v20+8*2048, tl=147456-(lane&3)*16384+wc*2048;
    unsigned t2; { int u1=suu+(pn&1); u1=u1==7?0:u1; t2=(spr*224+u1*32)*2048; }
    auto lin=[&](int L){ return (unsigned)(((kq+1+L)&3)*128); };
    auto issue_2=[&](int s){ unsigned so=__builtin_amdgcn_readlane(t2,s), l=147456-s*16384+wc*2048;
#pragma unroll
      for(int L=0;L<4;L++){ dma1(r2,l+L*4096,v20,so+lin(L)); dma1(r2,l+L*4096+1024,v21,so+lin(L)); } };
#pragma unroll 1
    for(int s=4;s<6;s++){ unsigned so=unit1(s)*(32*7168), l=133120-(s-2)*14336;
#pragma unroll
      for(int L=0;L<3;L++){ dma1(r1,l+L*4096+wc*2048,v10,so+L*128); dma1(r1,l+L*4096+wc*2048+1024,v11,so+L*128); }
      dma1(r1,l+12288+wc*1024,v1h,so); }
    base();
    bar();
    loop_b<1>(6,32,r1,utab*(32*7168),133120-((lane+6)&7)*14336+wc*2048,v10,v11,v1h,12288-wc*1024);
#pragma unroll 1
    for(int s=0;s<4;s++){ bar(); issue_2(s); waitvm<29>(); }
    bar(); bar();
    bar();
    waitvm<16>(); bar();
    loop_b<2>(4,20,r2,t2+lin(0),tl,v20,v21,0,lin(1)-lin(0),lin(2)-lin(0),lin(3)-lin(0));
#pragma unroll 1
    for(int s=20;s<28;s++){ const int u=20+4*(s&1); const unsigned o=__builtin_amdgcn_readlane(t2,u);
      loop_b<2>(s,s+1,r2,o+lin((s-20)>>1),tl,v20,v21,0,__builtin_amdgcn_readlane(t2,u+1)-o,__builtin_amdgcn_readlane(t2,u+2)-o,__builtin_amdgcn_readlane(t2,u+3)-o); }
    bar(); waitvm<8>();
  }
  const int w=wid&3;
  unsigned so=WS_XP+((pm*16+pn)*12+w)*4096, fo=WS_FX+(pm*16+pn)*512+w*128, vo=(wr*64+r)*2048+(g&1)*32+(g>>1)*16, oo=pm*128*2048+pn*128+wc*64;
  const rsrc_t rout=mkr(kp[10],2048*2048);
  asm volatile("":"+v"(vo)); asm volatile("":"+s"(so),"+s"(fo),"+s"(oo));
  bar(); waitvm<0>();
  bar();
  u32x4 pv[4],pw[4];
  auto fetch=[&](u32x4(&v)[4],int L){ wait_flags(rw,fo+L*8,0,tag);
#pragma unroll
    for(int m=0;m<4;m++) v[m]=ld128(rw,l16+m*1024,so+L*16384);
    fence(); };
  auto accum=[&](const u32x4(&v)[4]){
#pragma unroll
    for(int m=0;m<4;m++){
#pragma unroll
      for(int k=0;k<4;k++) c[4*m+k]=fma2(gate(k),up2(v[m][k]),c[4*m+k]);
      asm volatile("":"+v"(c[4*m]),"+v"(c[4*m+1]),"+v"(c[4*m+2]),"+v"(c[4*m+3])::"memory"); } };
  fetch(pv,0); fetch(pw,1);
  accum(pv);
  bar();
#pragma unroll
  for(int i=0;i<8;i++){ f32x4 v=*(LAS f32x4*)(L_OWN+w*8192+i*1024+l16); const int k=4*(i&3)+2*(i>>2);
    c[k]=fma2(gate(k&3),half2(v,0),c[k]); c[k+1]=fma2(gate((k+1)&3),half2(v,1),c[k+1]); asm volatile("":"+v"(c[k]),"+v"(c[k+1])); }
  fetch(pv,2);
  accum(pw);
#pragma unroll
  for(int m=0;m<4;m++){ unsigned o[4];
#pragma unroll
    for(int k=0;k<4;k++) o[k]=pk2(fma2(gate(k),up2(pv[m][k]),c[4*m+k]));
    auto s0=__builtin_amdgcn_permlane16_swap(o[0],o[2],false,false); auto s1=__builtin_amdgcn_permlane16_swap(o[1],o[3],false,false);
    u32x4 v={s0[0],s1[0],s0[1],s1[1]};
    st128(rout,v,vo,oo+m*32768); }
}

extern "C" void kernel_launch(void* const* d_in,const int* in_sizes,int n_in,void* d_out,int out_size,void* d_ws,size_t ws_size,hipStream_t stream){
  (void)in_sizes;(void)n_in;(void)out_size;(void)ws_size;
  (void)hipFuncSetAttribute((const void*)adaln_block,hipFuncAttributeMaxDynamicSharedMemorySize,163840);
  adaln_block<<<256,512,163840,stream>>>((const u16*)d_in[0],(const u16*)d_in[1],(const u16*)d_in[2],(const u16*)d_in[3],(const u16*)d_in[5],(const u16*)d_in[6],
    (const u16*)d_in[7],(const u16*)d_in[8],(const u16*)d_in[9],(const u16*)d_in[10],(u16*)d_out,(char*)d_ws);
}
```
